```python
import torch

def _softmax(x, axis=-1):
    return torch.softmax(x, dim=axis)

def _gelu(x):
    return torch.nn.functional.gelu(x, approximate='tanh')

def _silu(x):
    return torch.nn.functional.silu(x)

def _sigmoid(x):
    return torch.sigmoid(x)

def _layernorm(x, axis=-1, eps=1e-05):
    m = x.mean(dim=axis, keepdim=True)
    v = x.var(dim=axis, keepdim=True, unbiased=False)
    return (x - m) / torch.sqrt(v + eps)

def _scatter_add(tgt, idx, src, axis=0):
    return tgt.clone().index_add_(axis, idx, src)

def _segment_sum(src, seg, n):
    out = torch.zeros((n,) + src.shape[1:], dtype=src.dtype)
    return out.index_add_(0, seg, src)

def _pad(x, pw):
    flat = tuple((v for pair in reversed(pw) for v in pair))
    return torch.nn.functional.pad(x, flat)
kernel_name = 'parallel_sum__grouped_kv_mqa__mixer_gmlp__prompt'

def setup_inputs():

    _w = lambda a: (lambda t: t.to(torch.bfloat16) if t.is_floating_point() else t)(torch.as_tensor(a))

    def _A_setup():
        B, S, D, H = (1, 2048, 640, 32)
        Dh = D // H
        return {'x': _w((lambda *s: torch.randn(*s, dtype=torch.bfloat16) * 0.05)(B, S, D)), 'wq': _w((lambda *s: torch.randn(*s, dtype=torch.bfloat16) * 0.2)(D, H * Dh)), 'wk': _w((lambda *s: torch.randn(*s, dtype=torch.bfloat16) * 0.2)(D, Dh)), 'wv': _w((lambda *s: torch.randn(*s, dtype=torch.bfloat16) * 0.1)(D, Dh)), 'wo': _w((lambda *s: torch.randn(*s, dtype=torch.bfloat16) * 0.1)(H * Dh, D))}
    _A_in = _A_setup()

    def _B_setup():
        B, S, D, F = (1, 2048, 640, 1728)
        Fh = F // 2
        return {'x': _w((lambda *s: torch.randn(*s, dtype=torch.bfloat16) * 0.05)(B, S, D)), 'w_in': _w((lambda *s: torch.randn(*s, dtype=torch.bfloat16) * 0.8)(D, 2 * Fh)), 'w_sp': _w(0.01 * (lambda *s: torch.randn(*s, dtype=torch.bfloat16) * 0.8)(S, S)), 'b_sp': _w(1.0 + 0.01 * (lambda *s: torch.randn(*s, dtype=torch.bfloat16) * 0.1)(S)), 'w_out': _w((lambda *s: torch.randn(*s, dtype=torch.bfloat16) * 0.1)(Fh, D))}
    _B_in = _B_setup()
    return {'A_x': _A_in['x'], 'A_wq': _A_in['wq'], 'A_wk': _A_in['wk'], 'A_wv': _A_in['wv'], 'A_wo': _A_in['wo'], 'B_x': _B_in['x'], 'B_w_in': _B_in['w_in'], 'B_w_sp': _B_in['w_sp'], 'B_b_sp': _B_in['b_sp'], 'B_w_out': _B_in['w_out']}

def reference(A_x, A_wq, A_wk, A_wv, A_wo, B_x, B_w_in, B_w_sp, B_b_sp, B_w_out):

    def _A_ref(x, wq, wk, wv, wo):
        B, S, D = x.shape
        H = 32
        Dh = D // H
        q = (lambda x, *d: x.permute(*d))((x @ wq).reshape(B, S, H, Dh), 0, 2, 1, 3)
        k = x @ wk
        v = x @ wv
        scores = torch.einsum('bhqd,bkd->bhqk', q, k) * Dh ** (-0.5)
        p = _softmax(scores, axis=-1)
        o = torch.einsum('bhqk,bkd->bhqd', p, v)
        return (lambda x, *d: x.permute(*d))(o, 0, 2, 1, 3).reshape(B, S, H * Dh) @ wo

    def _B_ref(x, w_in, w_sp, b_sp, w_out):
        B, S, D = x.shape
        F = w_in.shape[-1]
        Fh = F // 2
        z = _gelu(torch.einsum('bsd,df->bsf', x, w_in))
        z1 = z[..., :Fh].clone()
        z2 = _layernorm(z[..., Fh:], axis=-1)
        g = torch.einsum('ts,bsf->btf', w_sp, z2) + b_sp[None, :, None]
        return torch.einsum('bsf,fd->bsd', z1 * g, w_out)
    x = A_x
    xn = _layernorm(x, axis=-1)
    return x + _A_ref(xn, A_wq, A_wk, A_wv, A_wo) + _B_ref(xn, B_w_in, B_w_sp, B_b_sp, B_w_out)
```

```cpp
#include <hip/hip_runtime.h>
#include <hip/hip_bf16.h>
typedef unsigned short u16;
__device__ __forceinline__ float bf2f(u16 b){ return __uint_as_float(((unsigned)b)<<16); }
__device__ __forceinline__ u16 f2bf(float f){ unsigned u=__float_as_uint(f); u += 0x7fffu + ((u>>16)&1u); return (u16)(u>>16); }
constexpr int S=2048, D=640, H=32, DH=20, F=1728, FH=864;

__global__ void k_ln(const u16* x, float* xn){
  int r=blockIdx.x; __shared__ float red[256];
  float s=0; for(int i=threadIdx.x;i<D;i+=256) s+=bf2f(x[r*D+i]);
  red[threadIdx.x]=s; __syncthreads(); for(int o=128;o>0;o>>=1){ if(threadIdx.x<o) red[threadIdx.x]+=red[threadIdx.x+o]; __syncthreads(); }
  float m=red[0]/D; __syncthreads();
  float v=0; for(int i=threadIdx.x;i<D;i+=256){ float d=bf2f(x[r*D+i])-m; v+=d*d; }
  red[threadIdx.x]=v; __syncthreads(); for(int o=128;o>0;o>>=1){ if(threadIdx.x<o) red[threadIdx.x]+=red[threadIdx.x+o]; __syncthreads(); }
  float rs=rsqrtf(red[0]/D+1e-5f);
  for(int i=threadIdx.x;i<D;i+=256) xn[r*D+i]=(bf2f(x[r*D+i])-m)*rs;
}
__global__ void k_gemm(const float* A, int lda, const u16* B, int ldb, float* C, int ldc, int M, int N, int K){
  __shared__ float As[16][17], Bs[16][17];
  int tx=threadIdx.x&15, ty=threadIdx.x>>4; int row=blockIdx.y*16+ty, col=blockIdx.x*16+tx; float acc=0;
  for(int k0=0;k0<K;k0+=16){
    As[ty][tx]=(row<M && k0+tx<K)?A[(size_t)row*lda+k0+tx]:0.f;
    Bs[ty][tx]=(k0+ty<K && col<N)?bf2f(B[(size_t)(k0+ty)*ldb+col]):0.f;
    __syncthreads();
    for(int k=0;k<16;++k) acc+=As[ty][k]*Bs[k][tx];
    __syncthreads();
  }
  if(row<M&&col<N) C[(size_t)row*ldc+col]=acc;
}
__global__ void k_gemm2(const u16* A, int lda, const float* B, int ldb, float* C, int ldc, int M, int N, int K){
  __shared__ float As[16][17], Bs[16][17];
  int tx=threadIdx.x&15, ty=threadIdx.x>>4; int row=blockIdx.y*16+ty, col=blockIdx.x*16+tx; float acc=0;
  for(int k0=0;k0<K;k0+=16){
    As[ty][tx]=(row<M && k0+tx<K)?bf2f(A[(size_t)row*lda+k0+tx]):0.f;
    Bs[ty][tx]=(k0+ty<K && col<N)?B[(size_t)(k0+ty)*ldb+col]:0.f;
    __syncthreads();
    for(int k=0;k<16;++k) acc+=As[ty][k]*Bs[k][tx];
    __syncthreads();
  }
  if(row<M&&col<N) C[(size_t)row*ldc+col]=acc;
}
__global__ void k_attn(const float* q,const float* k,const float* v,float* o){
  int idx=blockIdx.x*blockDim.x+threadIdx.x; int t=idx/H,h=idx%H;
  float qq[DH];
  #pragma unroll
  for(int d=0;d<DH;++d) qq[d]=q[t*D+h*DH+d]*0.22360679775f;
  float m=-1e30f,l=0,acc[DH];
  #pragma unroll
  for(int d=0;d<DH;++d) acc[d]=0;
  for(int j=0;j<S;++j){ float s=0;
    #pragma unroll
    for(int d=0;d<DH;++d) s+=qq[d]*k[j*DH+d];
    float mn=fmaxf(m,s); float a=__expf(m-mn), p=__expf(s-mn); l=l*a+p;
    #pragma unroll
    for(int d=0;d<DH;++d) acc[d]=acc[d]*a+p*v[j*DH+d]; m=mn; }
  #pragma unroll
  for(int d=0;d<DH;++d) o[t*D+h*DH+d]=acc[d]/l;
}
__global__ void k_gelu_ln(float* z){
  int r=blockIdx.x; __shared__ float red[256];
  for(int i=threadIdx.x;i<F;i+=256){ float x=z[r*F+i]; float u=0.7978845608f*(x+0.044715f*x*x*x); z[r*F+i]=bf2f(f2bf(0.5f*x*(1.f+tanhf(u)))); }
  __syncthreads();
  float s=0; for(int i=threadIdx.x;i<FH;i+=256) s+=z[r*F+FH+i];
  red[threadIdx.x]=s; __syncthreads(); for(int o=128;o>0;o>>=1){ if(threadIdx.x<o) red[threadIdx.x]+=red[threadIdx.x+o]; __syncthreads(); }
  float m=red[0]/FH; __syncthreads();
  float v=0; for(int i=threadIdx.x;i<FH;i+=256){ float d=z[r*F+FH+i]-m; v+=d*d; }
  red[threadIdx.x]=v; __syncthreads(); for(int o=128;o>0;o>>=1){ if(threadIdx.x<o) red[threadIdx.x]+=red[threadIdx.x+o]; __syncthreads(); }
  float rs=rsqrtf(red[0]/FH+1e-5f);
  for(int i=threadIdx.x;i<FH;i+=256) z[r*F+FH+i]=(z[r*F+FH+i]-m)*rs;
}
__global__ void k_gate(const float* z,const float* g,const u16* b,float* u){ int i=blockIdx.x*blockDim.x+threadIdx.x; if(i<S*FH){int t=i/FH,f=i%FH; u[i]=z[t*F+f]*(g[i]+bf2f(b[t]));} }
__global__ void k_final(const u16* x,const float* a,const float* b,u16* out){ int i=blockIdx.x*blockDim.x+threadIdx.x; if(i<S*D) out[i]=f2bf(bf2f(x[i])+a[i]+b[i]); }

extern "C" void kernel_launch(void* const* d_in, const int* in_sizes, int n_in, void* d_out, int out_size, void* d_ws, size_t ws_size, hipStream_t stream) {
  const u16* x=(const u16*)d_in[0]; const u16* wq=(const u16*)d_in[1]; const u16* wk=(const u16*)d_in[2]; const u16* wv=(const u16*)d_in[3]; const u16* wo=(const u16*)d_in[4];
  const u16* w_in=(const u16*)d_in[6]; const u16* w_sp=(const u16*)d_in[7]; const u16* b_sp=(const u16*)d_in[8]; const u16* w_out=(const u16*)d_in[9];
  float* ws=(float*)d_ws; float* xn=ws; float* q=xn+S*D; float* k=q+S*D; float* v=k+S*DH; float* o=v+S*DH; float* a=o+S*D; float* z=a+S*D; float* g=z+S*F; float* u=g+S*FH; float* b=u+S*FH;
  k_ln<<<S,256,0,stream>>>(x,xn);
  k_gemm<<<dim3(D/16,S/16),256,0,stream>>>(xn,D,wq,D,q,D,S,D,D);
  k_gemm<<<dim3(2,S/16),256,0,stream>>>(xn,D,wk,DH,k,DH,S,DH,D);
  k_gemm<<<dim3(2,S/16),256,0,stream>>>(xn,D,wv,DH,v,DH,S,DH,D);
  k_attn<<<S*H/128,128,0,stream>>>(q,k,v,o);
  k_gemm<<<dim3(D/16,S/16),256,0,stream>>>(o,D,wo,D,a,D,S,D,D);
  k_gemm<<<dim3(F/16,S/16),256,0,stream>>>(xn,D,w_in,F,z,F,S,F,D);
  k_gelu_ln<<<S,256,0,stream>>>(z);
  k_gemm2<<<dim3(FH/16,S/16),256,0,stream>>>(w_sp,S,z+FH,F,g,FH,S,FH,S);
  k_gate<<<(S*FH+255)/256,256,0,stream>>>(z,g,b_sp,u);
  k_gemm<<<dim3(D/16,S/16),256,0,stream>>>(u,FH,w_out,D,b,D,S,D,FH);
  k_final<<<(S*D+255)/256,256,0,stream>>>(x,a,b,(u16*)d_out);
}
```

```cpp
#include <hip/hip_runtime.h>
#include <stdint.h>
#pragma clang diagnostic ignored "-Winline-asm"

typedef unsigned short u16;
using bf16x8 = __attribute__((ext_vector_type(8))) __bf16;
using bf16x2 = __attribute__((ext_vector_type(2))) __bf16;
using s16x4 = __attribute__((ext_vector_type(4))) short;
using f32x2 = __attribute__((ext_vector_type(2))) float;
using f32x4 = __attribute__((ext_vector_type(4))) float;
using f32x16 = __attribute__((ext_vector_type(16))) float;
using u32x2 = __attribute__((ext_vector_type(2))) unsigned;
using u32x4 = __attribute__((ext_vector_type(4))) unsigned;
#define DI __device__ __forceinline__
#define LDSP __attribute__((address_space(3)))
typedef LDSP char* lds_ptr;

constexpr int S_ = 2048, D_ = 640, FH_ = 864, F_ = 1728;
constexpr size_t OFF_Q = 0;
constexpr size_t OFF_KF = OFF_Q + (size_t)S_ * D_ * 2;
constexpr size_t OFF_VF = OFF_KF + 131072;
constexpr size_t OFF_Z1 = OFF_VF + 131072;
constexpr size_t OFF_Z2R = OFF_Z1 + (size_t)S_ * FH_ * 2;
constexpr size_t OFF_Z2N = OFF_Z2R + (size_t)S_ * FH_ * 2;
constexpr size_t OFF_OU = OFF_Z2N + (size_t)S_ * FH_ * 2 + 4096;
constexpr size_t OFF_FLAG = OFF_OU + (size_t)S_ * 1536 * 2;
constexpr int KT_BYTES = 1536, VT_BYTES = 1344;

DI float bf2f(u16 b) { return __uint_as_float(((unsigned)b) << 16); }
DI unsigned pk2(float lo, float hi) { bf16x2 v = {(__bf16)lo, (__bf16)hi}; return __builtin_bit_cast(unsigned, v); }
DI float bflo(unsigned v) { return __uint_as_float(v << 16); }
DI float bfhi(unsigned v) { return __uint_as_float(v & 0xffff0000u); }
DI void glds16(const void* g, unsigned lds_base) {
    unsigned sv;
    asm volatile("s_mov_b32 %0, m0\n\ts_mov_b32 m0, %2\n\ts_nop 0\n\tglobal_load_lds_dwordx4 %1, off\n\ts_mov_b32 m0, %0"
                 : "=&s"(sv) : "v"(g), "s"(lds_base) : "memory");
}
DI void glds(const void* g, unsigned lds_dst) {
    asm volatile("s_mov_b32 m0, %1\n\ts_nop 0\n\tglobal_load_lds_dwordx4 %0, off" :: "v"(g), "s"(lds_dst) : "memory", "m0");
}
DI void glds_lo32(const void* g, unsigned lds_dst) {
    asm volatile("s_mov_b32 m0, %1\n\ts_mov_b32 exec_hi, 0\n\tglobal_load_lds_dwordx4 %0, off\n\ts_mov_b32 exec_hi, -1" :: "v"(g), "s"(lds_dst) : "memory", "m0");
}
typedef __amdgpu_buffer_rsrc_t rsrc_t;
DI rsrc_t mk_rsrc(const void* p, unsigned bytes) { return __builtin_amdgcn_make_buffer_rsrc((void*)p, 0, bytes, 0x00020000); }
DI void bdma(rsrc_t r, unsigned voff, unsigned soff, unsigned lds_dst) {
    asm volatile("s_mov_b32 m0, %3\n\ts_nop 0\n\tbuffer_load_dwordx4 %0, %1, %2 offen lds" :: "v"(voff), "s"(r), "s"(soff), "s"(lds_dst) : "memory", "m0");
}
#define WAIT_VM(n) asm volatile("s_waitcnt vmcnt(" #n ")" ::: "memory")
#define WAIT_BAR(n) asm volatile("s_waitcnt vmcnt(" #n ") lgkmcnt(0)\n\ts_barrier" ::: "memory")
#define BAR_LDS() asm volatile("s_waitcnt lgkmcnt(0)\n\ts_barrier" ::: "memory")
#define SBAR() __builtin_amdgcn_sched_barrier(0)
DI u32x4 ldsr128(lds_ptr p) { return *(const LDSP u32x4*)p; }
typedef short v4i16_t __attribute__((ext_vector_type(4)));
DI u32x2 ldstr(lds_ptr p) { return __builtin_bit_cast(u32x2, __builtin_amdgcn_ds_read_tr16_b64_v4i16((LDSP v4i16_t*)p)); }
DI f32x4 mfma16(u32x4 a, u32x4 b, f32x4 c) { return __builtin_amdgcn_mfma_f32_16x16x32_bf16(__builtin_bit_cast(bf16x8, a), __builtin_bit_cast(bf16x8, b), c, 0, 0, 0); }
DI void mfma16_acc(f32x4& acc, const u32x4& a, const u32x4& b) { asm volatile("v_mfma_f32_16x16x32_bf16 %0, %1, %2, %0" : "+v"(acc) : "v"(a), "v"(b)); }
DI f32x16 mfma32(u32x4 a, u32x4 b, f32x16 c) { return __builtin_amdgcn_mfma_f32_32x32x16_bf16(__builtin_bit_cast(bf16x8, a), __builtin_bit_cast(bf16x8, b), c, 0, 0, 0); }
DI f32x16 mfma32k8(u32x2 a, u32x2 b, f32x16 c) { return __builtin_amdgcn_mfma_f32_32x32x8bf16_1k(__builtin_bit_cast(s16x4, a), __builtin_bit_cast(s16x4, b), c, 0, 0, 0); }
DI float dot2(unsigned a, unsigned b, float c) { return __builtin_amdgcn_fdot2_f32_bf16(__builtin_bit_cast(bf16x2, a), __builtin_bit_cast(bf16x2, b), c, false); }
DI float gelu_tanh(float x) {
    const float c0 = -2.302208198f, c1 = -0.1029432397f;
    float e = __builtin_amdgcn_exp2f(x * __builtin_fmaf(x * x, c1, c0));
    return x * __builtin_amdgcn_rcpf(1.f + e);
}
#define STAMP_DECL
#define STAMP(i)
#define RSTAMP(i)
#define STAMP_FLUSH()
#define STAMP_ARG
#define STAMP_PASS
namespace k1 {
constexpr int BM = 160, A_BYTES = BM * 128, B_BYTES = 64 * 256, NKS = 10;
constexpr int L_A = 0, L_B = 4 * A_BYTES, L_ST = L_B + 4 * B_BYTES, LDS_BYTES = L_ST + 4096;
enum { T_Q = 0, T_K = 1, T_V = 2, T_Z1 = 3, T_Z2 = 4 };
struct Params { const u16* x; const u16* wq; const u16* wk; const u16* wv; const u16* win; char* ws; };

struct Ld { rsrc_t ra, rb; unsigned va[2], vb[2], sa, sb, sbinc, da, db; const char* pk[4]; unsigned pinc[4]; };
template <bool KV, int S> DI void ld_issue(Ld& L) {
#pragma unroll
    for (int j = 0; j < 5; ++j) bdma(L.ra, L.va[j & 1], L.sa + S * 128 + j * 8 * 1280, L.da + (S & 3) * A_BYTES + j * 1024);
#pragma unroll
    for (int j = 0; j < 4; ++j) {
        if constexpr (KV) { glds(L.pk[j], L.db + (S & 3) * B_BYTES + j * 1024); L.pk[j] += L.pinc[j]; asm volatile("" : "+v"(L.pk[j])); }
        else bdma(L.rb, L.vb[(j >> 1) & 1], L.sb + S * L.sbinc + j * (L.sbinc >> 4), L.db + (S & 3) * B_BYTES + j * 1024);
    }
}
template <bool KV, int S> DI void ld_step(Ld& L) {
    if constexpr (S + 2 < NKS) { ld_issue<KV, S + 2>(L); WAIT_BAR(9); } else WAIT_BAR(0);
    if constexpr (S + 2 < NKS) ld_step<KV, S + 1>(L);
}
template <bool KV> DI void loader(Ld& L STAMP_ARG) {
    ld_issue<KV, 0>(L); ld_issue<KV, 1>(L);
    STAMP(2);
    WAIT_BAR(9);
    STAMP(3);
    ld_step<KV, 0>(L);
}

struct Frag { u32x4 x[5], w[2]; };
template <int S, int KS> DI void rd(Frag& f, lds_ptr lds, const unsigned (&xo)[5], unsigned wo0, unsigned wo1) {
    constexpr int sa = (S & 3) * A_BYTES, sb = (S & 3) * B_BYTES + KS * 8192;
#pragma unroll
    for (int n = 0; n < 2; ++n) {
        lds_ptr wp = lds + (n ? wo1 : wo0) + sb;
        const u32x2 lo = ldstr(wp), hi = ldstr(wp + 1024);
        f.w[n] = u32x4{lo[0], lo[1], hi[0], hi[1]};
    }
#pragma unroll
    for (int m = 0; m < 5; ++m) f.x[m] = ldsr128(lds + (xo[m] ^ (KS * 64)) + sa);
}
struct Stat { f32x4 s0, q0, s1, q1, cs; };
template <int S, int KS, int I> DI void rd1(Frag& f, lds_ptr lds, const unsigned (&xo)[5], unsigned wo0, unsigned wo1) {
    constexpr int sa = (S & 3) * A_BYTES, sb = (S & 3) * B_BYTES + KS * 8192;
    if constexpr (I < 4) {
        const u32x2 t = ldstr(lds + ((I >> 1) ? wo1 : wo0) + sb + (I & 1) * 1024);
        f.w[I >> 1][2 * (I & 1)] = t[0]; f.w[I >> 1][2 * (I & 1) + 1] = t[1];
    } else f.x[I - 4] = ldsr128(lds + (xo[I - 4] ^ (KS * 64)) + sa);
}
template <bool FT, int KSG, bool RD, int S, int KS>
DI void mm(const Frag& fc, Frag& fn, lds_ptr lds, const unsigned (&xo)[5], unsigned wo0, unsigned wo1, f32x4 (&acc)[5][2], int wc, Stat& st, const u32x4& ones) {
#define K1_MM(i) do { if (FT) mfma16_acc(acc[(i) >> 1][(i) & 1], fc.w[(i) & 1], fc.x[(i) >> 1]); else mfma16_acc(acc[(i) >> 1][(i) & 1], fc.x[(i) >> 1], fc.w[(i) & 1]); } while (0)
#define K1_RD(i) do { if constexpr (RD) rd1<S, KS, i>(fn, lds, xo, wo0, wo1); SBAR(); } while (0)
    K1_MM(0); K1_RD(0); K1_MM(1); K1_RD(1); K1_MM(2); K1_RD(2); K1_MM(3); K1_RD(3); K1_MM(4); K1_RD(4);
    K1_MM(5); K1_RD(5); K1_MM(6); K1_RD(6); K1_MM(7); K1_RD(7); K1_MM(8); K1_RD(8); K1_MM(9);
#undef K1_MM
#undef K1_RD
    mfma16_acc(st.s0, fc.x[0], ones); mfma16_acc(st.q0, fc.x[0], fc.x[0]); mfma16_acc(st.cs, ones, fc.w[0]);
    if ((KSG & 3) == wc) { mfma16_acc(st.s1, fc.x[4], ones); mfma16_acc(st.q1, fc.x[4], fc.x[4]); }
    SBAR();
}
template <bool FT, int S>
DI void stage(lds_ptr lds, Frag& f0, Frag& f1, const unsigned (&xo)[5], unsigned wo0, unsigned wo1, int wc,
              f32x4 (&acc)[5][2], Stat& st, const u32x4& ones STAMP_ARG) {
    if constexpr (S + 1 < NKS) BAR_LDS();
    STAMP(3 + S);
    mm<FT, 2 * S, true, S, 1>(f0, f1, lds, xo, wo0, wo1, acc, wc, st, ones);
    mm<FT, 2 * S + 1, (S + 1 < NKS), S + 1, 0>(f1, f0, lds, xo, wo0, wo1, acc, wc, st, ones);
    if constexpr (S + 1 < NKS) stage<FT, S + 1>(lds, f0, f1, xo, wo0, wo1, wc, acc, st, ones STAMP_PASS);
}
template <bool FT>
DI void mainloop(lds_ptr lds, int lane, int wr, int wc, f32x4 (&acc)[5][2], Stat& st STAMP_ARG) {
    const int r15 = lane & 15, g = lane >> 4;
    const unsigned xb = L_A + (80 * wr + r15) * 128 + ((g ^ ((r15 >> 1) & 7)) << 4);
    unsigned xo[5];
#pragma unroll
    for (int m = 0; m < 4; ++m) xo[m] = xb + (m ^ wc) * 2048;
    xo[4] = xb + 8192;
    const int q = r15 >> 2, p = lane & 3;
    const int fk = (q << 1) | ((g & 1) << 3);
    const unsigned wo0 = (L_B + (8 * g + q) * 256 + (((4 * wc + (p >> 1)) ^ fk) << 4) + 8 * (p & 1)) ^ (wr * 32), wo1 = wo0 ^ 32;
    Frag f0, f1;
    u32x4 ones = u32x4{0x3F803F80u, 0x3F803F80u, 0x3F803F80u, 0x3F803F80u};
    asm volatile("" : "+v"(ones), "+v"(st.s0), "+v"(st.q0), "+v"(st.s1), "+v"(st.q1), "+v"(st.cs));
#pragma unroll
    for (int m = 0; m < 5; ++m) asm volatile("" : "+v"(acc[m][0]), "+v"(acc[m][1]));
    BAR_LDS();
    rd<0, 0>(f0, lds, xo, wo0, wo1);
    stage<FT, 0>(lds, f0, f1, xo, wo0, wo1, wc, acc, st, ones STAMP_PASS);
    asm volatile("s_nop 7\n\ts_nop 7" ::: "memory");
}
DI void gelu8(float (&v)[8]) {
    float t[8];
#pragma unroll
    for (int i = 0; i < 8; ++i) t[i] = v[i] * __builtin_fmaf(v[i] * v[i], -0.1029432397f, -2.302208198f);
#pragma unroll
    for (int i = 0; i < 8; ++i) t[i] = __builtin_amdgcn_exp2f(t[i]);
#pragma unroll
    for (int i = 0; i < 8; ++i) t[i] = __builtin_amdgcn_rcpf(1.f + t[i]);
#pragma unroll
    for (int i = 0; i < 8; ++i) v[i] *= t[i];
}
template <int TYPE>
DI void epilogue(char* ws, const f32x4 (&acc)[5][2], const LDSP float* csum, const LDSP float* ra, const LDSP float* rbv, int tw, int col0, int lane, int wr, int wc) {
    const int r15 = lane & 15, g = lane >> 4;
    constexpr bool FT = (TYPE == T_Q || TYPE == T_K || TYPE == T_Z1);
    float cf[2][4];
#pragma unroll
    for (int n = 0; n < 2; ++n) {
        if constexpr (FT) { const f32x4 c4 = *(const LDSP f32x4*)(csum + 32 * wc + 16 * (n ^ wr) + 4 * g); for (int j = 0; j < 4; ++j) cf[n][j] = c4[j]; }
        else cf[n][0] = csum[32 * wc + 16 * (n ^ wr) + r15];
    }
    f32x4 av[5], bv[5];
#pragma unroll
    for (int mp = 0; mp < 5; ++mp) {
        const int m = mp < 4 ? (mp ^ wc) : 4;
        if constexpr (FT) { av[mp][0] = ra[80 * wr + 16 * m + r15]; bv[mp][0] = rbv[80 * wr + 16 * m + r15]; }
        else { av[mp] = *(const LDSP f32x4*)(ra + 80 * wr + 16 * m + 4 * g); bv[mp] = *(const LDSP f32x4*)(rbv + 80 * wr + 16 * m + 4 * g); }
    }
#pragma unroll
    for (int mp = 0; mp < 5; ++mp) {
        const int m = mp < 4 ? (mp ^ wc) : 4;
        const int tb = tw + 16 * m;
        float v[8];
#pragma unroll
        for (int n = 0; n < 2; ++n)
#pragma unroll
            for (int j = 0; j < 4; ++j) {
                if constexpr (FT) { constexpr float sc = (TYPE == T_Q) ? 0.32259534218f : 1.f;
                                    v[4 * n + j] = __builtin_fmaf(av[mp][0], acc[mp][n][j], bv[mp][0] * cf[n][j]) * sc; }
                else v[4 * n + j] = __builtin_fmaf(av[mp][j], acc[mp][n][j], bv[mp][j] * cf[n][0]);
            }
        if constexpr (TYPE == T_Z1 || TYPE == T_Z2) gelu8(v);
        const unsigned pa0 = pk2(v[0], v[1]), pa1 = pk2(v[2], v[3]), pb0 = pk2(v[4], v[5]), pb1 = pk2(v[6], v[7]);
        const unsigned t00 = wr ? pb0 : pa0, t01 = wr ? pb1 : pa1, t10 = wr ? pa0 : pb0, t11 = wr ? pa1 : pb1;
        if (tb < S_) {
            if constexpr (TYPE == T_K) {
                const int t = tb + r15;
                char* kt = ws + OFF_KF + (t >> 5) * KT_BYTES + (t & 31) * 16;
                *(u32x2*)(kt + (g >> 1) * 512 + (g & 1) * 8) = u32x2{t00, t01};
                if (g == 1) *(u32x2*)(kt + 1024) = u32x2{t10, t11};
                if (g == 2) *(u32x2*)(kt + 1024 + 8) = u32x2{0x00003F80u, 0u};
            } else if constexpr (TYPE == T_V) {
                char* vt = ws + OFF_VF + (tb >> 5) * VT_BYTES + ((((tb >> 4) & 1) * 2 + (g & 1)) * 21) * 16 + (g >> 1) * 8;
                *(u32x2*)(vt + r15 * 16) = u32x2{t00, t01};
                if (r15 >= 4 && r15 < 8) *(u32x2*)(vt + (12 + r15) * 16) = u32x2{t10, t11};
                if (r15 == 8) *(u32x2*)(vt + 20 * 16) = u32x2{0x3F803F80u, 0x3F803F80u};
            } else {
                auto x0 = __builtin_amdgcn_permlane16_swap(t00, t10, false, false);
                auto x1 = __builtin_amdgcn_permlane16_swap(t01, t11, false, false);
                char* dst;
                if constexpr (TYPE == T_Z2) dst = ws + OFF_Z2R + (unsigned)(((tb >> 3) + (g >> 1)) * FH_ + col0 + 16 * (g & 1) + r15) * 16;
                else dst = ws + (TYPE == T_Q ? OFF_Q : OFF_Z1) + (unsigned)((tb + r15) * (TYPE == T_Q ? 1280 : 1728) + (col0 + 16 * (g & 1) + 8 * (g >> 1)) * 2);
                *(u32x4*)dst = u32x4{x0[0], x1[0], x0[1], x1[1]};
            }
        }
    }
}

__global__ __launch_bounds__(768, 3) void k1_proj(Params P) {
    extern __shared__ __attribute__((aligned(16))) char smem[];
    const int tid = threadIdx.x, lane = tid & 63, w = __builtin_amdgcn_readfirstlane(tid >> 6);
    STAMP_DECL; STAMP(0); RSTAMP(30);
    const int b = blockIdx.x, j0 = b >> 3, n0 = (b & 7) * 31 + j0;
    if (j0 == 31 || n0 >= 247) {
        if (b == 255 && tid < 64) ((u32x4*)(P.ws + OFF_FLAG))[tid] = u32x4{0u, 0u, 0u, 0u};
        return;
    }
    int rb, cb;
    if (n0 < 133) { cb = n0 / 7; rb = n0 - cb * 7; } else { const int m = n0 - 133; cb = m / 6; rb = 7 + m - cb * 6; }
    const int row0 = rb * BM;
    lds_ptr lds = (lds_ptr)smem;
    const unsigned lds0 = (unsigned)(uintptr_t)smem;
    if (w >= 8) {
        const int L = w - 8;
        Ld ld;
        ld.ra = mk_rsrc(P.x, S_ * 1280);
        const int r8 = lane >> 3, c8 = lane & 7, k4 = lane >> 4, c16 = lane & 15;
#pragma unroll
        for (int i = 0; i < 2; ++i) ld.va[i] = r8 * 1280 + ((c8 ^ ((4 * ((L + i) & 1) + k4) & 7)) << 4);
        ld.sa = (row0 + 40 * L) * 1280;
        ld.da = lds0 + L_A + 5 * L * 1024; ld.db = lds0 + L_B + 4 * L * 1024;
        const unsigned ldw = cb < 5 ? 1280u : 3456u;
        ld.rb = cb < 5 ? mk_rsrc(P.wq, D_ * 1280) : mk_rsrc(P.win, D_ * 3456);
#pragma unroll
        for (int i = 0; i < 2; ++i) ld.vb[i] = k4 * ldw + ((c16 ^ (k4 << 1) ^ (i << 3)) << 4);
        ld.sb = (cb < 5 ? cb : cb - 5) * 256 + 16 * L * ldw; ld.sbinc = 64 * ldw;
        STAMP(1);
        if (cb != 18) loader<false>(ld STAMP_PASS);
        else {
#pragma unroll
            for (int j = 0; j < 4; ++j) {
                const int k = 16 * L + 4 * j + k4, c = c16 ^ (k4 << 1) ^ (((j >> 1) & 1) << 3), cc = c & 3;
                if (c < 8) { ld.pk[j] = (const char*)P.win + k * 3456 + 13 * 256 + c * 16; ld.pinc[j] = 64 * 3456; }
                else { ld.pk[j] = (const char*)(c < 12 ? P.wk : P.wv) + k * 40 + (cc == 1 ? 16 : cc == 2 ? 24 : 0); ld.pinc[j] = 64 * 40; }
            }
            loader<true>(ld STAMP_PASS);
        }
        return;
    }
    const int wr = w & 1, wc = w >> 1;
    int type, col0;
    if (cb < 5) { type = T_Q; col0 = cb * 128 + wc * 32; }
    else { const int zc = (cb - 5) * 128 + wc * 32;
           if (cb == 18 && wc >= 2) { type = (wc == 2) ? T_K : T_V; col0 = 0; }
           else if (zc < FH_) { type = T_Z1; col0 = zc; } else { type = T_Z2; col0 = zc - FH_; } }
    const bool ft = (type == T_Q || type == T_K || type == T_Z1);
    f32x4 acc[5][2];
#pragma unroll
    for (int m = 0; m < 5; ++m) { acc[m][0] = f32x4{0, 0, 0, 0}; acc[m][1] = f32x4{0, 0, 0, 0}; }
    Stat st; st.s0 = st.q0 = st.s1 = st.q1 = st.cs = f32x4{0, 0, 0, 0};
    STAMP(2);
    if (ft) mainloop<true>(lds, lane, wr, wc, acc, st STAMP_PASS);
    else mainloop<false>(lds, lane, wr, wc, acc, st STAMP_PASS);
    STAMP(13);
    const int r15 = lane & 15, g = lane >> 4;
    LDSP float* rsum = (LDSP float*)(lds + L_ST); LDSP float* rsq = rsum + 160; LDSP float* csum = rsum + 320; LDSP float* ra = rsum + 448; LDSP float* rbv = rsum + 608;
    LDSP float* p4s = rsum + 768; LDSP float* p4q = rsum + 896;
    if (r15 == 0) { *(LDSP f32x4*)(rsum + 80 * wr + 16 * wc + 4 * g) = st.s0; *(LDSP f32x4*)(p4s + wc * 32 + wr * 16 + 4 * g) = st.s1; }
    if ((r15 >> 2) == g) {
        const int j = r15 & 3;
        rsq[80 * wr + 16 * wc + r15] = j == 0 ? st.q0[0] : j == 1 ? st.q0[1] : j == 2 ? st.q0[2] : st.q0[3];
        p4q[wc * 32 + wr * 16 + r15] = j == 0 ? st.q1[0] : j == 1 ? st.q1[1] : j == 2 ? st.q1[2] : st.q1[3];
    }
    if (g == 0) csum[32 * wc + 16 * wr + r15] = st.cs[0];
    BAR_LDS();
    if (tid < BM) {
        const int tr = tid >= 80, tl = tid - 80 * tr, o = tr * 16 + (tl & 15);
        float sm = rsum[tid], sq = rsq[tid];
        const float s4 = p4s[o] + p4s[32 + o] + p4s[64 + o] + p4s[96 + o], q4 = p4q[o] + p4q[32 + o] + p4q[64 + o] + p4q[96 + o];
        if (tl >= 64) { sm = s4; sq = q4; }
        const float mu = sm * (1.f / D_), var = fmaxf(sq * (1.f / D_) - mu * mu, 0.f);
        const float rs = __builtin_amdgcn_rsqf(var + 1e-5f);
        ra[tid] = rs; rbv[tid] = -mu * rs;
    }
    BAR_LDS();
    STAMP(14);
    const int tw = row0 + 80 * wr;
    switch (type) {
        case T_Q: epilogue<T_Q>(P.ws, acc, csum, ra, rbv, tw, col0, lane, wr, wc); break;
        case T_K: epilogue<T_K>(P.ws, acc, csum, ra, rbv, tw, col0, lane, wr, wc); break;
        case T_V: epilogue<T_V>(P.ws, acc, csum, ra, rbv, tw, col0, lane, wr, wc); break;
        case T_Z1: epilogue<T_Z1>(P.ws, acc, csum, ra, rbv, tw, col0, lane, wr, wc); break;
        default: epilogue<T_Z2>(P.ws, acc, csum, ra, rbv, tw, col0, lane, wr, wc); break;
    }
    STAMP(15); RSTAMP(31); STAMP_FLUSH();
}
}
namespace k2 {
constexpr int KV_HALF = 12288, KV_STAGE = 2 * KV_HALF, W_STAGE = 32768;
constexpr int L_KV = 0, L_W = 2 * KV_STAGE, L_RED = L_W + 2 * W_STAGE, LDS_BYTES = L_RED + 1024;
constexpr float M_SHIFT = 90.f;
constexpr int ZSTEP = 2 * FH_ * 16;
struct Params { const u16* wsp; const u16* bsp; char* ws; };
#define USE(x) asm volatile("" :: "v"(x))
#define EXP2(x) __builtin_amdgcn_exp2f(x)
DI float rowmax32(const f32x16& s) {
    float a = fmaxf(fmaxf(s[0], s[1]), s[2]), b = fmaxf(fmaxf(s[3], s[4]), s[5]);
#pragma unroll
    for (int r = 6; r < 14; r += 4) { a = fmaxf(fmaxf(a, s[r]), s[r + 1]); b = fmaxf(fmaxf(b, s[r + 2]), s[r + 3]); }
    a = fmaxf(fmaxf(a, s[14]), s[15]); a = fmaxf(a, b);
    auto rr = __builtin_amdgcn_permlane32_swap(__float_as_uint(a), __float_as_uint(a), false, false);
    return fmaxf(__uint_as_float(rr[0]), __uint_as_float(rr[1]));
}
DI void pack16(const f32x16& s, u32x4& p0, u32x4& p1) {
    p0 = u32x4{pk2(s[0], s[1]), pk2(s[2], s[3]), pk2(s[4], s[5]), pk2(s[6], s[7])};
    p1 = u32x4{pk2(s[8], s[9]), pk2(s[10], s[11]), pk2(s[12], s[13]), pk2(s[14], s[15])};
}
DI f32x16 attn_fallback(const char* ws, u32x4 q0, u32x2 q1, int lane) {
    const int r = lane & 31, h = lane >> 5;
    if (h) q1 = u32x2{0u, 0u};
    f32x16 o = {}; float m = -INFINITY;
#pragma unroll 1
    for (int T = 0; T < 64; ++T) {
        const char* kt = ws + OFF_KF + T * KT_BYTES; const char* vt = ws + OFF_VF + T * VT_BYTES;
        const u32x4 k0 = *(const u32x4*)(kt + h * 512 + r * 16); const u32x2 k1 = *(const u32x2*)(kt + 1024 + r * 16 + h * 8);
        const int rv = min(r, 20);
        const u32x4 v0 = *(const u32x4*)(vt + (h * 21 + rv) * 16), v1 = *(const u32x4*)(vt + ((2 + h) * 21 + rv) * 16);
        f32x16 s = mfma32(k0, q0, f32x16{}); s = mfma32k8(k1, q1, s);
        const float mn = fmaxf(m, rowmax32(s));
        const float al = (mn == -INFINITY) ? 1.f : EXP2(m - mn);
        const float ms = (mn == -INFINITY) ? 0.f : mn;
#pragma unroll
        for (int i = 0; i < 16; ++i) { s[i] = EXP2(s[i] - ms); o[i] *= al; }
        m = mn;
        u32x4 p0, p1; pack16(s, p0, p1);
        o = mfma32(v0, p0, o); o = mfma32(v1, p1, o);
    }
    return o;
}

struct St {
    u32x4 qa0, qb0; u32x2 qa1, qb1;
    f32x16 sa, sb, oa, ob;
    u32x4 pa0, pa1, pb0, pb1;
    u32x4 k0[2]; u32x2 k1[2]; u32x4 v0[2], v1[2];
};
#define K2_SCHED 4
#define E_(S, i) S[i] = EXP2(S[i]); USE(S[i])
#define C_(P, j, S, i) P[j] = pk2(S[i], S[i + 1]); USE(P[j])
#define K2_STEP(S, P0, P1, SP, PP1, M0, M1, M2, M3, R0, R1, R2)                                          \
    do {                                                                                                 \
        C_(PP1, 3, SP, 14); M0; R0; SBAR(); E_(S, 0); E_(S, 1); E_(S, 2); E_(S, 3); SBAR();              \
        M1; R1; C_(P0, 0, S, 0); E_(S, 4); E_(S, 5); C_(P0, 1, S, 2); E_(S, 6); E_(S, 7); SBAR();        \
        M2; R2; C_(P0, 2, S, 4); E_(S, 8); E_(S, 9); C_(P0, 3, S, 6); E_(S, 10); E_(S, 11); SBAR();      \
        M3; C_(P1, 0, S, 8); E_(S, 12); E_(S, 13); C_(P1, 1, S, 10); E_(S, 14); E_(S, 15); C_(P1, 2, S, 12); SBAR(); \
    } while (0)
#define K2_FLIP 2
template <int STEP> DI void prio_step() {
    if constexpr (STEP == 0) __builtin_amdgcn_s_setprio(3);
    if constexpr (STEP == 2) __builtin_amdgcn_s_setprio(2);
    if constexpr (STEP == 4) __builtin_amdgcn_s_setprio(1);
    if constexpr (STEP == 6) __builtin_amdgcn_s_setprio(0);
}
template <int I>
DI void att_iter(St& s, lds_ptr pk0, lds_ptr pk1, lds_ptr pv) {
    constexpr int c = I & 1, n = c ^ 1;
    prio_step<2 * I>();
    K2_STEP(s.sa, s.pa0, s.pa1, s.sb, s.pb1,
            s.sb = mfma32(s.k0[c], s.qb0, f32x16{}),
            s.sb = mfma32k8(s.k1[c], s.qb1, s.sb),
            s.ob = mfma32(s.v0[n], s.pb0, s.ob),
            s.ob = mfma32(s.v1[n], s.pb1, s.ob),
            { s.k0[n] = ldsr128(pk0 + I * KT_BYTES); s.k1[n] = *(const LDSP u32x2*)(pk1 + I * KT_BYTES); },
            { s.v0[c] = ldsr128(pv + I * VT_BYTES); },
            { s.v1[c] = ldsr128(pv + I * VT_BYTES + 672); });
    prio_step<2 * I + 1>();
    K2_STEP(s.sb, s.pb0, s.pb1, s.sa, s.pa1,
            s.sa = mfma32(s.k0[n], s.qa0, f32x16{}),
            s.sa = mfma32k8(s.k1[n], s.qa1, s.sa),
            s.oa = mfma32(s.v0[c], s.pa0, s.oa),
            s.oa = mfma32(s.v1[c], s.pa1, s.oa),
            {}, {}, {});
}
DI void att_loop(St& s, lds_ptr kvb, unsigned k0o, unsigned k1o, unsigned vo STAMP_ARG) {
#pragma unroll 1
    for (int ss = 0; ss < 8; ++ss) {
        BAR_LDS();
        lds_ptr kv = kvb + (ss & 1) * KV_STAGE;
        att_iter<0>(s, kv + k0o, kv + k1o, kv + vo); att_iter<1>(s, kv + k0o, kv + k1o, kv + vo);
        att_iter<2>(s, kv + k0o, kv + k1o, kv + vo); att_iter<3>(s, kv + k0o, kv + k1o, kv + vo);
    }
}
DI void attention(const Params& P, lds_ptr lds, int b, int w, int lane STAMP_ARG) {
    const int p = w & 3, hf = w >> 2, r31 = lane & 31, h = lane >> 5;
    char* ws = P.ws;
    St s;
    const int tA = 8 * b + 2 * p;
    const char* qa = ws + OFF_Q + (unsigned)(tA * 1280 + r31 * 40);
    s.qa0 = *(const u32x4*)(qa + h * 16); s.qb0 = *(const u32x4*)(qa + 1280 + h * 16);
    s.qa1 = *(const u32x2*)(qa + 32); s.qb1 = *(const u32x2*)(qa + 1280 + 32);
    const char* kt0 = ws + OFF_KF;
    const u32x4 k00 = *(const u32x4*)(kt0 + h * 512 + r31 * 16); const u32x2 k01 = *(const u32x2*)(kt0 + 1024 + r31 * 16 + h * 8);
    const char* ktf = ws + OFF_KF + hf * 32 * KT_BYTES;
    s.k0[0] = *(const u32x4*)(ktf + h * 512 + r31 * 16); s.k1[0] = *(const u32x2*)(ktf + 1024 + r31 * 16 + h * 8);
    if (h) { s.qa1 = u32x2{0u, 0u}; s.qb1 = u32x2{0u, 0u}; }
    {
        f32x16 a = mfma32(k00, s.qa0, f32x16{}); a = mfma32k8(k01, s.qa1, a);
        f32x16 c = mfma32(k00, s.qb0, f32x16{}); c = mfma32k8(k01, s.qb1, c);
        const float ma = rowmax32(a) + M_SHIFT, mb = rowmax32(c) + M_SHIFT;
        if (h) { s.qa1[0] = pk2(-ma, 0.f); s.qb1[0] = pk2(-mb, 0.f); }
    }
    s.sa = mfma32(s.k0[0], s.qa0, f32x16{}); s.sa = mfma32k8(s.k1[0], s.qa1, s.sa);
    s.oa = f32x16{}; s.ob = f32x16{}; s.sb = f32x16{};
    s.pb0 = u32x4{0, 0, 0, 0}; s.pb1 = u32x4{0, 0, 0, 0}; s.v0[1] = u32x4{0, 0, 0, 0}; s.v1[1] = u32x4{0, 0, 0, 0};
    lds_ptr kvb = lds + L_KV + hf * KV_HALF;
    const unsigned k0o = h * 512 + r31 * 16, k1o = 1024 + r31 * 16 + h * 8, vo = 6144 + (h * 21 + r31) * 16;
    STAMP(3);
    att_loop(s, kvb, k0o, k1o, vo STAMP_PASS);
    STAMP(15);
    s.pb1[3] = pk2(s.sb[14], s.sb[15]);
    s.ob = mfma32(s.v0[1], s.pb0, s.ob); s.ob = mfma32(s.v1[1], s.pb1, s.ob);
    BAR_LDS();
    LDSP f32x4* xs = (LDSP f32x4*)lds + w * 3 * 64;
    LDSP f32x4* xr = (LDSP f32x4*)lds + (w ^ 4) * 3 * 64;
    if (hf == 0) {
#pragma unroll
        for (int i = 0; i < 3; ++i) xs[i * 64 + lane] = f32x4{s.ob[4 * i], s.ob[4 * i + 1], s.ob[4 * i + 2], s.ob[4 * i + 3]};
    } else {
#pragma unroll
        for (int i = 0; i < 3; ++i) xs[i * 64 + lane] = f32x4{s.oa[4 * i], s.oa[4 * i + 1], s.oa[4 * i + 2], s.oa[4 * i + 3]};
        s.oa = s.ob; s.qa0 = s.qb0; s.qa1 = s.qb1;
    }
    BAR_LDS();
    STAMP(16);
    f32x16 o = s.oa;
#pragma unroll
    for (int i = 0; i < 3; ++i) { const f32x4 t = xr[i * 64 + lane]; o[4 * i] += t[0]; o[4 * i + 1] += t[1]; o[4 * i + 2] += t[2]; o[4 * i + 3] += t[3]; }
    auto lr = __builtin_amdgcn_permlane32_swap(__float_as_uint(o[8]), __float_as_uint(o[8]), false, false);
    float l = __uint_as_float(lr[1]);
    bool ok = (l >= 0x1p-110f) && (l <= 0x1p100f);
#pragma unroll
    for (int i = 0; i < 12; ++i) ok = ok && (fabsf(o[i]) <= 0x1p126f || (h && i >= 8));
    if (__builtin_expect(!__all(ok), 0)) {
        o = attn_fallback(ws, s.qa0, s.qa1, lane);
        lr = __builtin_amdgcn_permlane32_swap(__float_as_uint(o[8]), __float_as_uint(o[8]), false, false);
        l = __uint_as_float(lr[1]);
    }
    const float rl = 1.f / l;
    char* op = ws + OFF_OU + (unsigned)((tA + hf) * 3072 + r31 * 40 + h * 8);
    *(u32x2*)(op) = u32x2{pk2(o[0] * rl, o[1] * rl), pk2(o[2] * rl, o[3] * rl)};
    *(u32x2*)(op + 16) = u32x2{pk2(o[4] * rl, o[5] * rl), pk2(o[6] * rl, o[7] * rl)};
    if (!h) *(u32x2*)(op + 32) = u32x2{pk2(o[8] * rl, o[9] * rl), pk2(o[10] * rl, o[11] * rl)};
}

DI void zload(u32x4& z, rsrc_t r, unsigned voff, unsigned soff) {
    asm volatile("buffer_load_dwordx4 %0, %1, %2, %3 offen sc1" : "+v"(z) : "v"(voff), "s"(r), "s"(soff) : "memory");
}
#define ZWAIT(n, z) asm volatile("s_waitcnt vmcnt(" #n ")" : "+v"(z) :: "memory")
struct Gs {
    rsrc_t rkv, rw, rz; unsigned vkv, vw[8], skv[6], sw, dkv, dw, zoff;
    u32x4 z[16]; f32x16 g0, g1; u32x4 wa0, wa1;
};
template <int I> DI void kv_dma(const Gs& g) { bdma(g.rkv, g.vkv, g.skv[I], g.dkv + I * 1024); }
template <int I> DI void w_dma(const Gs& g) { bdma(g.rw, g.vw[I], g.sw + 2 * I * 4096, g.dw + I * 1024); }
template <int I, bool MM, bool DMA_KV, bool DMA_W, bool ZL>
DI void g_step(Gs& g, lds_ptr wp, unsigned wx, unsigned zs) {
    if constexpr (DMA_KV && I < 6) kv_dma<I>(g);
    if constexpr (DMA_W && I < 6) { w_dma<I>(g); if constexpr (I < 2) w_dma<6 + I>(g); }
    if constexpr (MM) {
        u32x4 wn0, wn1;
        if constexpr (I == 0) { g.wa0 = ldsr128(wp + wx); g.wa1 = ldsr128(wp + wx + 16384); }
        if constexpr (I < 15) { wn0 = ldsr128(wp + (wx ^ ((I + 1) * 32))); wn1 = ldsr128(wp + (wx ^ ((I + 1) * 32)) + 16384); }
        if constexpr (ZL) ZWAIT(29, g.z[I]);
        g.g0 = mfma32(g.z[I], g.wa0, g.g0); g.g1 = mfma32(g.z[I], g.wa1, g.g1);
        if constexpr (ZL) zload(g.z[I], g.rz, g.zoff, zs + I * ZSTEP);
        if constexpr (I < 15) { g.wa0 = wn0; g.wa1 = wn1; }
        SBAR();
    }
    if constexpr (I < 15) g_step<I + 1, MM, DMA_KV, DMA_W, ZL>(g, wp, wx, zs);
}
template <bool HASG>
DI void gating(const Params& P, lds_ptr lds, unsigned lds0, int b, int gw, int lane, int t0, int f0 STAMP_ARG) {
    char* ws = P.ws;
    const int t256 = gw * 64 + lane, r31 = lane & 31, h = lane >> 5;
    Gs g;
    g.rkv = mk_rsrc(ws + OFF_KF, (unsigned)(OFF_Z1 - OFF_KF)); g.rw = mk_rsrc(P.wsp, S_ * 4096); g.rz = mk_rsrc(ws + OFF_Z2N, (unsigned)(S_ * FH_ * 2 + 4096));
    g.vkv = lane * 16;
    unsigned kvinc;
    {
        const int ph = gw >> 1;
        const unsigned base = (gw & 1) ? (unsigned)(OFF_VF - OFF_KF) + (ph * 32) * VT_BYTES : (ph * 32 + 1) * KT_BYTES;
        kvinc = (gw & 1) ? 4 * VT_BYTES : 4 * KT_BYTES;
#pragma unroll
        for (int i = 0; i < 6; ++i) g.skv[i] = base + i * 1024;
    }
    const unsigned kvdst = lds0 + L_KV + 6 * gw * 1024;
    auto next_kv = [&]() {
#pragma unroll
        for (int i = 0; i < 6; ++i) g.skv[i] += kvinc;
    };
    g.dkv = kvdst;
    g_step<0, false, true, false, false>(g, lds, 0, 0); next_kv();
    const char* zr0 = ws + OFF_Z2R + (unsigned)(b * FH_ * 16);
    u32x4 c[4];
#pragma unroll
    for (int i = 0; i < 4; ++i) c[i] = *(const u32x4*)(zr0 + (i < 3 || t256 < FH_ - 768 ? t256 + 256 * i : t256) * 16);
    if (t256 >= FH_ - 768) c[3] = u32x4{0, 0, 0, 0};
    LDSP float* red = (LDSP float*)(lds + L_RED);
    LDSP unsigned* cnt = (LDSP unsigned*)(lds + L_RED + 512);
    {
        float v[16];
#pragma unroll
        for (int j = 0; j < 8; ++j) { v[j] = 0.f; v[8 + j] = 0.f; }
#pragma unroll
        for (int i = 0; i < 4; ++i)
#pragma unroll
            for (int j = 0; j < 4; ++j) {
                const float lo = bflo(c[i][j]), hi = bfhi(c[i][j]);
                v[2 * j] += lo; v[2 * j + 1] += hi; v[8 + 2 * j] = __builtin_fmaf(lo, lo, v[8 + 2 * j]); v[8 + 2 * j + 1] = __builtin_fmaf(hi, hi, v[8 + 2 * j + 1]);
            }
        float u[8], t[4];
#pragma unroll
        for (int j = 0; j < 8; ++j) { auto r = __builtin_amdgcn_permlane32_swap(__float_as_uint(v[j]), __float_as_uint(v[j + 8]), false, false); u[j] = __uint_as_float(r[0]) + __uint_as_float(r[1]); }
#pragma unroll
        for (int j = 0; j < 4; ++j) { auto r = __builtin_amdgcn_permlane16_swap(__float_as_uint(u[j]), __float_as_uint(u[j + 4]), false, false); t[j] = __uint_as_float(r[0]) + __uint_as_float(r[1]); }
#pragma unroll
        for (int j = 0; j < 4; ++j) {
            t[j] += __uint_as_float(__builtin_amdgcn_update_dpp(0, __float_as_uint(t[j]), 0x128, 0xf, 0xf, false));
            t[j] += __uint_as_float(__builtin_amdgcn_update_dpp(0, __float_as_uint(t[j]), 0x124, 0xf, 0xf, false));
            t[j] += __uint_as_float(__builtin_amdgcn_update_dpp(0, __float_as_uint(t[j]), 0x122, 0xf, 0xf, false));
            t[j] += __uint_as_float(__builtin_amdgcn_update_dpp(0, __float_as_uint(t[j]), 0x121, 0xf, 0xf, false));
        }
        if ((lane & 15) == 0) { const int rho = lane >> 4; *(LDSP f32x4*)(red + gw * 16 + (rho >> 1) * 8 + (rho & 1) * 4) = f32x4{t[0], t[1], t[2], t[3]}; }
        if (gw == 0 && lane == 0) { cnt[0] = 0u; cnt[1] = 0u; }
    }
    STAMP(2);
    WAIT_BAR(0);
    STAMP(4);
    g.dkv = kvdst + KV_STAGE;
    g_step<0, false, true, false, false>(g, lds, 0, 0); next_kv();
#pragma unroll
    for (int i = 0; i < 8; ++i) g.vw[i] = (lane >> 5) * 4096 + (((lane & 31) ^ ((2 * i + (lane >> 5)) & 15)) << 4);
    g.sw = (t0 + 16 * gw) * 4096;
    const unsigned wdst = lds0 + L_W + 8 * gw * 1024;
    if (HASG) { g.dw = wdst; g_step<0, false, false, true, false>(g, lds, 0, 0); g.sw += 512; }
    {
        float sm = 0, sq = 0;
#pragma unroll
        for (int j = 0; j < 4; ++j) { sm += red[j * 16 + (lane & 7)]; sq += red[j * 16 + 8 + (lane & 7)]; }
        const float mu = sm * (1.f / FH_), var = fmaxf(sq * (1.f / FH_) - mu * mu, 0.f), rs = __builtin_amdgcn_rsqf(var + 1e-5f), sh = -mu * rs;
        float a[8], bb[8];
#pragma unroll
        for (int j = 0; j < 8; ++j) { a[j] = __uint_as_float(__builtin_amdgcn_readlane(__float_as_uint(rs), j)); bb[j] = __uint_as_float(__builtin_amdgcn_readlane(__float_as_uint(sh), j)); }
        const rsrc_t zw = mk_rsrc(ws + OFF_Z2N + (unsigned)(b * FH_ * 16), FH_ * 16);
#pragma unroll
        for (int i = 0; i < 4; ++i) {
            u32x4 o;
#pragma unroll
            for (int j = 0; j < 4; ++j) o[j] = pk2(__builtin_fmaf(bflo(c[i][j]), a[2 * j], bb[2 * j]), __builtin_fmaf(bfhi(c[i][j]), a[2 * j + 1], bb[2 * j + 1]));
            __builtin_amdgcn_raw_buffer_store_b128(o, zw, (t256 + 256 * i) * 16, 0, 16);
        }
    }
    WAIT_VM(0);
    unsigned* flags = (unsigned*)(ws + OFF_FLAG);
    if (lane == 0) {
        const unsigned old = __hip_atomic_fetch_add(cnt, 1u, __ATOMIC_RELAXED, __HIP_MEMORY_SCOPE_WORKGROUP);
        if (old == 3u) __hip_atomic_store(flags + b, 1u, __ATOMIC_RELAXED, __HIP_MEMORY_SCOPE_AGENT);
    }
    STAMP(5);
    g.zoff = (h * FH_ + f0 + r31) * 16;
    g.g0 = f32x16{}; g.g1 = f32x16{};
#pragma unroll
    for (int i = 0; i < 16; ++i) g.z[i] = u32x4{0, 0, 0, 0};
    lds_ptr wb = lds + L_W;
    const unsigned wx = r31 * 512 + (((lane & 15) ^ h) << 4);
    BAR_LDS();
    g.dkv = kvdst; g.dw = wdst + W_STAGE;
    g_step<0, false, true, HASG, false>(g, lds, 0, 0); next_kv(); g.sw += 512;
    if (HASG) {
        if (gw == 0) {
            const u32x4* fp = (const u32x4*)flags + lane;
            for (unsigned spin = 0; spin < (1u << 22); ++spin) {
                u32x4 f;
                asm volatile("global_load_dwordx4 %0, %1, off sc1\n\ts_waitcnt vmcnt(0)" : "=&v"(f) : "v"(fp) : "memory");
                if (__all(f[0] == 1u && f[1] == 1u && f[2] == 1u && f[3] == 1u)) break;
                __builtin_amdgcn_s_sleep(2);
            }
            if (lane == 0) __hip_atomic_store(cnt + 1, 1u, __ATOMIC_RELAXED, __HIP_MEMORY_SCOPE_WORKGROUP);
        } else {
            for (unsigned spin = 0; spin < (1u << 24); ++spin) {
                if (__hip_atomic_load(cnt + 1, __ATOMIC_RELAXED, __HIP_MEMORY_SCOPE_WORKGROUP) != 0u) break;
                __builtin_amdgcn_s_sleep(1);
            }
        }
        asm volatile("" ::: "memory");
        STAMP(6);
#pragma unroll
        for (int i = 0; i < 16; ++i) zload(g.z[i], g.rz, g.zoff, i * ZSTEP);
#pragma unroll
        for (int i = 0; i < 16; ++i) ZWAIT(0, g.z[i]);
        g_step<0, true, false, false, false>(g, wb, wx, 0);
#pragma unroll
        for (int i = 0; i < 16; ++i) zload(g.z[i], g.rz, g.zoff, (16 + i) * ZSTEP);
    }
#pragma unroll 1
    for (int ss = 2; ss < 8; ++ss) {
        if (HASG) { if (ss == 2) WAIT_BAR(16); else WAIT_BAR(11); } else WAIT_BAR(0);
        g.dkv = kvdst + ((ss + 1) & 1) * KV_STAGE; g.dw = wdst + (ss & 1) * W_STAGE;
        if (HASG) {
            if (ss == 2) asm volatile("s_waitcnt vmcnt(0)" : "+v"(g.z[0]), "+v"(g.z[1]), "+v"(g.z[2]), "+v"(g.z[3]) :: "memory");
            g_step<0, true, true, true, true>(g, wb + ((ss - 1) & 1) * W_STAGE, wx, (ss * 16) * ZSTEP);
        } else g_step<0, false, true, false, false>(g, lds, 0, 0);
        next_kv(); g.sw += 512;
    }
    u32x2 z1v[2][4]; float bias[2] = {0.f, 0.f};
    if (HASG && f0 < FH_) {
#pragma unroll
        for (int u = 0; u < 2; ++u) {
            const int t = t0 + 32 * u + r31;
            const char* z1p = ws + OFF_Z1 + (unsigned)(t * 1728 + (f0 + 4 * h) * 2);
#pragma unroll
            for (int i = 0; i < 4; ++i) z1v[u][i] = *(const u32x2*)(z1p + i * 16);
            bias[u] = bf2f(P.bsp[t]);
        }
    }
    asm volatile("s_waitcnt vmcnt(0) lgkmcnt(0)\n\ts_barrier\n\ts_barrier"
                 : "+v"(g.z[0]), "+v"(g.z[1]), "+v"(g.z[2]), "+v"(g.z[3]), "+v"(g.z[4]), "+v"(g.z[5]), "+v"(g.z[6]), "+v"(g.z[7]),
                   "+v"(g.z[8]), "+v"(g.z[9]), "+v"(g.z[10]), "+v"(g.z[11]), "+v"(g.z[12]), "+v"(g.z[13]), "+v"(g.z[14]), "+v"(g.z[15]) :: "memory");
    STAMP(15);
    if (HASG) {
        g_step<0, true, false, false, false>(g, wb + W_STAGE, wx, 0);
        STAMP(16);
#pragma unroll
        for (int u = 0; u < 2; ++u) {
            const int t = t0 + 32 * u + r31;
            char* up = ws + OFF_OU + (unsigned)(t * 3072 + (640 + f0 + 4 * h) * 2);
            const f32x16& gg = u ? g.g1 : g.g0;
#pragma unroll
            for (int i = 0; i < 4; ++i) {
                u32x2 o = u32x2{0u, 0u};
                if (f0 < FH_) {
                    const u32x2 z1 = z1v[u][i];
                    o = u32x2{pk2(bflo(z1[0]) * (gg[4 * i] + bias[u]), bfhi(z1[0]) * (gg[4 * i + 1] + bias[u])),
                              pk2(bflo(z1[1]) * (gg[4 * i + 2] + bias[u]), bfhi(z1[1]) * (gg[4 * i + 3] + bias[u]))};
                }
                *(u32x2*)(up + i * 16) = o;
            }
        }
    }
}

__global__ __launch_bounds__(768, 3) void k2_mix(Params P) {
    extern __shared__ __attribute__((aligned(16))) char smem[];
    const int lane = threadIdx.x & 63, w = __builtin_amdgcn_readfirstlane(threadIdx.x >> 6);
    STAMP_DECL; STAMP(0); RSTAMP(30);
    const int b = blockIdx.x, j = b >> 3;
    lds_ptr lds = (lds_ptr)smem; const unsigned lds0 = (unsigned)(uintptr_t)smem;
    if (w < 8) attention(P, lds, b, w, lane STAMP_PASS);
    else if (j < 28) { const int rbg = (b & 7) * 4 + j / 7, cbg = j % 7; gating<true>(P, lds, lds0, b, w - 8, lane, rbg * 64, cbg * 128 + (w - 8) * 32 STAMP_PASS); }
    else gating<false>(P, lds, lds0, b, w - 8, lane, 0, 0 STAMP_PASS);
    STAMP(17); RSTAMP(31); STAMP_FLUSH();
}
}
namespace k3 {
constexpr int BM = 96, A_BYTES = BM * 128, STAGE = 20480, NST = 7, NKS = 24, LDS_BYTES = NST * STAGE;
struct Params { const u16* x; const u16* wo; const u16* wout; const char* ws; u16* out; };
struct Ld { rsrc_t ra, rb0, rb1; unsigned va[2], vb[2], sa, sb, da, db; };
template <int S> DI void ld_issue(const Ld& L) {
    constexpr int so = (S % NST) * STAGE;
#pragma unroll
    for (int j = 0; j < 3; ++j) bdma(L.ra, L.va[j & 1], L.sa + S * 128 + j * 8 * 3072, L.da + so + j * 1024);
#pragma unroll
    for (int j = 0; j < 2; ++j) bdma(S < 10 ? L.rb0 : L.rb1, L.vb[j], L.sb + ((S < 10 ? S : S - 10) * 64 + j * 8) * 1280, L.db + so + j * 1024);
}
template <int S> DI void ld_step(const Ld& L) {
    constexpr int issued = (S + NST - 2 < NKS) ? S + NST - 2 : NKS - 1;
    if constexpr (S + NST - 2 < NKS && S > 0) ld_issue<S + NST - 2>(L);
    constexpr int young = 5 * (issued - (S + 1));
    if constexpr (young == 20) WAIT_BAR(20); else if constexpr (young == 15) WAIT_BAR(15); else if constexpr (young == 10) WAIT_BAR(10);
    else if constexpr (young == 5) WAIT_BAR(5); else WAIT_BAR(0);
    if constexpr (S + 2 < NKS) ld_step<S + 1>(L);
}
struct Frag { u32x4 x[3], w[2]; };
struct Ctx { unsigned xo[3], wo0[3], wo1[3]; };
template <int S> DI void rd(Frag& f, lds_ptr lds, const Ctx& c) {
    constexpr int slot = S % NST, grp = slot / 3, so = (slot % 3) * STAGE;
#pragma unroll
    for (int n = 0; n < 2; ++n) {
        lds_ptr wp = lds + (n ? c.wo1[grp] : c.wo0[grp]) + so;
        const u32x2 lo = ldstr(wp), hi = ldstr(wp + 512);
        f.w[n] = u32x4{lo[0], lo[1], hi[0], hi[1]};
    }
#pragma unroll
    for (int m = 0; m < 3; ++m) f.x[m] = ldsr128(lds + c.xo[grp] + so + m * 2048);
}
template <int S> DI void stage(lds_ptr lds, const Ctx& c, Frag& fc, Frag& fn, f32x4 (&acc)[3][2] STAMP_ARG) {
    if constexpr (S + 1 < NKS) {
        BAR_LDS();
        STAMP(2 + S);
        rd<S + 1>(fn, lds, c);
        SBAR();
    }
#pragma unroll
    for (int m = 0; m < 3; ++m)
#pragma unroll
        for (int n = 0; n < 2; ++n) acc[m][n] = mfma16(fc.w[n], fc.x[m], acc[m][n]);
    SBAR();
    if constexpr (S + 1 < NKS) stage<S + 1>(lds, c, fn, fc, acc STAMP_PASS);
}

__global__ __launch_bounds__(768, 3) void k3_out(Params P) {
    extern __shared__ __attribute__((aligned(16))) char smem[];
    const int tid = threadIdx.x, lane = tid & 63, w = __builtin_amdgcn_readfirstlane(tid >> 6);
    STAMP_DECL; STAMP(0); RSTAMP(30);
    const int b = blockIdx.x, j0 = b >> 3, n0 = (b & 7) * 28 + j0;
    if (j0 >= 28 || n0 >= 220) return;
    const int half = n0 >= 110, r_ = n0 - half * 110, rb = r_ / 5, cb = half * 5 + r_ - rb * 5;
    const int row0 = rb * BM;
    lds_ptr lds = (lds_ptr)smem;
    const unsigned lds0 = (unsigned)(uintptr_t)smem;
    if (w >= 8) {
        const int L = w - 8, r8 = lane >> 3, c8 = lane & 7, k4 = lane >> 4;
        Ld ld;
        ld.ra = mk_rsrc(P.ws + OFF_OU, S_ * 3072); ld.rb0 = mk_rsrc(P.wo, D_ * 1280); ld.rb1 = mk_rsrc(P.wout, FH_ * 1280);
#pragma unroll
        for (int i = 0; i < 2; ++i) {
            ld.va[i] = r8 * 3072 + ((c8 ^ ((4 * ((L + i) & 1) + k4) & 7)) << 4);
            ld.vb[i] = r8 * 1280 + ((c8 ^ (((k4 & 1) | (i << 1)) << 1)) << 4);
        }
        ld.sa = (row0 + 24 * L) * 3072; ld.sb = cb * 128 + 16 * L * 1280;
        ld.da = lds0 + 3 * L * 1024; ld.db = lds0 + A_BYTES + 2 * L * 1024;
        STAMP(1);
        ld_issue<0>(ld); ld_issue<1>(ld); ld_issue<2>(ld);
        STAMP(2);
        WAIT_BAR(10);
        STAMP(3);
        ld_issue<3>(ld); ld_issue<4>(ld); ld_issue<5>(ld);
        STAMP(4);
        ld_step<0>(ld);
        return;
    }
    const int kh = w >> 2, wr = w & 1, wc = (w >> 1) & 1;
    const int r15 = lane & 15, g = lane >> 4, q = r15 >> 2, p = lane & 3;
    const int co = cb * 64 + wc * 32;
    u32x2 xr[2][2];
#pragma unroll
    for (int u = 0; u < 2; ++u) {
        const int m = kh ? 2 : u, t = min(row0 + 48 * wr + 16 * m + r15, S_ - 1);
#pragma unroll
        for (int n = 0; n < 2; ++n) xr[u][n] = *(const u32x2*)(P.x + (unsigned)(t * D_ + co + 16 * n + 4 * g));
    }
    Ctx c;
    const unsigned xoff = (48 * wr + r15) * 128 + (((4 * kh + g) ^ ((r15 >> 1) & 7)) << 4);
    const int fk = (((q >> 1) & 1) | ((g & 1) << 1)) << 1;
    const unsigned woff = A_BYTES + (32 * kh + 8 * g + q) * 128 + (((4 * wc + (p >> 1)) ^ fk) << 4) + 8 * (p & 1);
#pragma unroll
    for (int i = 0; i < 3; ++i) { c.xo[i] = xoff + i * 3 * STAGE; c.wo0[i] = woff + i * 3 * STAGE; c.wo1[i] = (woff ^ 32) + i * 3 * STAGE; }
    f32x4 acc[3][2];
#pragma unroll
    for (int m = 0; m < 3; ++m) { acc[m][0] = f32x4{0, 0, 0, 0}; acc[m][1] = f32x4{0, 0, 0, 0}; }
    Frag f0, f1;
    BAR_LDS();
    rd<0>(f0, lds, c);
    stage<0>(lds, c, f0, f1, acc STAMP_PASS);
    STAMP(28);
    BAR_LDS();
    LDSP f32x4* xch = (LDSP f32x4*)lds + (w & 3) * 6 * 64;
    if (kh == 1) {
#pragma unroll
        for (int m = 0; m < 2; ++m) { xch[(m * 2) * 64 + lane] = acc[m][0]; xch[(m * 2 + 1) * 64 + lane] = acc[m][1]; }
    } else { xch[4 * 64 + lane] = acc[2][0]; xch[5 * 64 + lane] = acc[2][1]; }
    BAR_LDS();
#pragma unroll
    for (int u = 0; u < 2; ++u) {
        if (kh == 1 && u == 1) break;
        const int m = kh ? 2 : u;
        const int tb = row0 + 48 * wr + 16 * m;
        if (tb >= S_) break;
        const int t = tb + r15;
        unsigned pk[2][2];
#pragma unroll
        for (int n = 0; n < 2; ++n) {
            const f32x4 mine = kh ? acc[2][n] : acc[u][n];
            const f32x4 v = mine + xch[(m * 2 + n) * 64 + lane];
            pk[n][0] = pk2(v[0] + bflo(xr[u][n][0]), v[1] + bfhi(xr[u][n][0])); pk[n][1] = pk2(v[2] + bflo(xr[u][n][1]), v[3] + bfhi(xr[u][n][1]));
        }
        auto x0 = __builtin_amdgcn_permlane16_swap(pk[0][0], pk[1][0], false, false);
        auto x1 = __builtin_amdgcn_permlane16_swap(pk[0][1], pk[1][1], false, false);
        *(u32x4*)(P.out + (unsigned)(t * D_ + co + 16 * (g & 1) + 8 * (g >> 1))) = u32x4{x0[0], x1[0], x0[1], x1[1]};
    }
    STAMP(29); RSTAMP(31); STAMP_FLUSH();
}
}
extern "C" void kernel_launch(void* const* d_in, const int* in_sizes, int n_in, void* d_out, int out_size, void* d_ws, size_t ws_size,
                              hipStream_t stream) {
    (void)in_sizes; (void)n_in; (void)out_size; (void)ws_size;
    const u16* x = (const u16*)d_in[0];
    k1::Params p1{x, (const u16*)d_in[1], (const u16*)d_in[2], (const u16*)d_in[3], (const u16*)d_in[6], (char*)d_ws};
    k2::Params p2{(const u16*)d_in[7], (const u16*)d_in[8], (char*)d_ws};
    k3::Params p3{x, (const u16*)d_in[4], (const u16*)d_in[9], (const char*)d_ws, (u16*)d_out};
    (void)hipFuncSetAttribute((const void*)k1::k1_proj, hipFuncAttributeMaxDynamicSharedMemorySize, k1::LDS_BYTES);
    (void)hipFuncSetAttribute((const void*)k2::k2_mix, hipFuncAttributeMaxDynamicSharedMemorySize, k2::LDS_BYTES);
    (void)hipFuncSetAttribute((const void*)k3::k3_out, hipFuncAttributeMaxDynamicSharedMemorySize, k3::LDS_BYTES);
    k1::k1_proj<<<256, 768, k1::LDS_BYTES, stream>>>(p1);
    k2::k2_mix<<<256, 768, k2::LDS_BYTES, stream>>>(p2);
    k3::k3_out<<<256, 768, k3::LDS_BYTES, stream>>>(p3);
}
```

```cpp
#include <hip/hip_runtime.h>
#include <stdint.h>
#pragma clang diagnostic ignored "-Winline-asm"

typedef unsigned short u16;
using bf16x8 = __attribute__((ext_vector_type(8))) __bf16;
using bf16x2 = __attribute__((ext_vector_type(2))) __bf16;
using s16x4 = __attribute__((ext_vector_type(4))) short;
using f32x2 = __attribute__((ext_vector_type(2))) float;
using f32x4 = __attribute__((ext_vector_type(4))) float;
using f32x16 = __attribute__((ext_vector_type(16))) float;
using u32x2 = __attribute__((ext_vector_type(2))) unsigned;
using u32x4 = __attribute__((ext_vector_type(4))) unsigned;
#define DI __device__ __forceinline__
#define LDSP __attribute__((address_space(3)))
typedef LDSP char* lds_ptr;

constexpr int S_ = 2048, D_ = 640, FH_ = 864, F_ = 1728;
constexpr size_t OFF_Q = 0;
constexpr size_t OFF_KF = OFF_Q + (size_t)S_ * D_ * 2;
constexpr size_t OFF_VF = OFF_KF + 131072;
constexpr size_t OFF_Z1 = OFF_VF + 131072;
constexpr size_t OFF_Z2R = OFF_Z1 + (size_t)S_ * FH_ * 2;
constexpr size_t OFF_Z2N = OFF_Z2R + (size_t)S_ * FH_ * 2;
constexpr size_t OFF_OU = OFF_Z2N + (size_t)S_ * FH_ * 2 + 4096;
constexpr size_t OFF_FLAG = OFF_OU + (size_t)S_ * 1536 * 2;
constexpr int V_SLOTS = 22;
constexpr int KT_BYTES = 1536, VT_BYTES = 4 * V_SLOTS * 16;

DI float bf2f(u16 b) { return __uint_as_float(((unsigned)b) << 16); }
DI unsigned pk2(float lo, float hi) { bf16x2 v = {(__bf16)lo, (__bf16)hi}; return __builtin_bit_cast(unsigned, v); }
DI float bflo(unsigned v) { return __uint_as_float(v << 16); }
DI float bfhi(unsigned v) { return __uint_as_float(v & 0xffff0000u); }
DI void glds16(const void* g, unsigned lds_base) {
    unsigned sv;
    asm volatile("s_mov_b32 %0, m0\n\ts_mov_b32 m0, %2\n\ts_nop 0\n\tglobal_load_lds_dwordx4 %1, off\n\ts_mov_b32 m0, %0"
                 : "=&s"(sv) : "v"(g), "s"(lds_base) : "memory");
}
DI void glds(const void* g, unsigned lds_dst) {
    asm volatile("s_mov_b32 m0, %1\n\ts_nop 0\n\tglobal_load_lds_dwordx4 %0, off" :: "v"(g), "s"(lds_dst) : "memory", "m0");
}
DI void glds_lo32(const void* g, unsigned lds_dst) {
    asm volatile("s_mov_b32 m0, %1\n\ts_mov_b32 exec_hi, 0\n\tglobal_load_lds_dwordx4 %0, off\n\ts_mov_b32 exec_hi, -1" :: "v"(g), "s"(lds_dst) : "memory", "m0");
}
typedef __amdgpu_buffer_rsrc_t rsrc_t;
DI rsrc_t mk_rsrc(const void* p, unsigned bytes) { return __builtin_amdgcn_make_buffer_rsrc((void*)p, 0, bytes, 0x00020000); }
DI void bdma(rsrc_t r, unsigned voff, unsigned soff, unsigned lds_dst) {
    asm volatile("s_mov_b32 m0, %3\n\ts_nop 0\n\tbuffer_load_dwordx4 %0, %1, %2 offen lds" :: "v"(voff), "s"(r), "s"(soff), "s"(lds_dst) : "memory", "m0");
}
#define WAIT_VM(n) asm volatile("s_waitcnt vmcnt(" #n ")" ::: "memory")
#define WAIT_BAR(n) asm volatile("s_waitcnt vmcnt(" #n ") lgkmcnt(0)\n\ts_barrier" ::: "memory")
#define BAR_LDS() asm volatile("s_waitcnt lgkmcnt(0)\n\ts_barrier" ::: "memory")
#define SBAR() __builtin_amdgcn_sched_barrier(0)
DI u32x4 ldsr128(lds_ptr p) { return *(const LDSP u32x4*)p; }
typedef short v4i16_t __attribute__((ext_vector_type(4)));
DI u32x2 ldstr(lds_ptr p) { return __builtin_bit_cast(u32x2, __builtin_amdgcn_ds_read_tr16_b64_v4i16((LDSP v4i16_t*)p)); }
#define ST_AUX 16
DI void st128(rsrc_t r, unsigned off, u32x4 v) { __builtin_amdgcn_raw_buffer_store_b128(v, r, off, 0, ST_AUX); }
DI void st64(rsrc_t r, unsigned off, u32x2 v) { __builtin_amdgcn_raw_buffer_store_b64(v, r, off, 0, ST_AUX); }
DI void touch_lines8(rsrc_t r, unsigned voff, unsigned soff, unsigned step) {
    unsigned t0, t1, t2, t3, t4, t5, t6, t7;
    asm volatile("buffer_load_dword %0, %8, %9, %10 offen\n\tbuffer_load_dword %1, %8, %9, %11 offen\n\tbuffer_load_dword %2, %8, %9, %12 offen\n\t"
                 "buffer_load_dword %3, %8, %9, %13 offen\n\tbuffer_load_dword %4, %8, %9, %14 offen\n\tbuffer_load_dword %5, %8, %9, %15 offen\n\t"
                 "buffer_load_dword %6, %8, %9, %16 offen\n\tbuffer_load_dword %7, %8, %9, %17 offen\n\ts_waitcnt vmcnt(0)"
                 : "=&v"(t0), "=&v"(t1), "=&v"(t2), "=&v"(t3), "=&v"(t4), "=&v"(t5), "=&v"(t6), "=&v"(t7)
                 : "v"(voff), "s"(r), "s"(soff), "s"(soff + step), "s"(soff + 2 * step), "s"(soff + 3 * step), "s"(soff + 4 * step), "s"(soff + 5 * step),
                   "s"(soff + 6 * step), "s"(soff + 7 * step) : "memory");
}
DI f32x4 mfma16(u32x4 a, u32x4 b, f32x4 c) { return __builtin_amdgcn_mfma_f32_16x16x32_bf16(__builtin_bit_cast(bf16x8, a), __builtin_bit_cast(bf16x8, b), c, 0, 0, 0); }
DI void mfma16_acc(f32x4& acc, const u32x4& a, const u32x4& b) { asm volatile("v_mfma_f32_16x16x32_bf16 %0, %1, %2, %0" : "+v"(acc) : "v"(a), "v"(b)); }
DI f32x16 mfma32(u32x4 a, u32x4 b, f32x16 c) { return __builtin_amdgcn_mfma_f32_32x32x16_bf16(__builtin_bit_cast(bf16x8, a), __builtin_bit_cast(bf16x8, b), c, 0, 0, 0); }
DI f32x16 mfma32k8(u32x2 a, u32x2 b, f32x16 c) { return __builtin_amdgcn_mfma_f32_32x32x8bf16_1k(__builtin_bit_cast(s16x4, a), __builtin_bit_cast(s16x4, b), c, 0, 0, 0); }
DI float dot2(unsigned a, unsigned b, float c) { return __builtin_amdgcn_fdot2_f32_bf16(__builtin_bit_cast(bf16x2, a), __builtin_bit_cast(bf16x2, b), c, false); }
DI float gelu_tanh(float x) {
    const float c0 = -2.302208198f, c1 = -0.1029432397f;
    float e = __builtin_amdgcn_exp2f(x * __builtin_fmaf(x * x, c1, c0));
    return x * __builtin_amdgcn_rcpf(1.f + e);
}
#define STAMP_DECL
#define STAMP(i)
#define RSTAMP(i)
#define STAMP_FLUSH_K(k)
#define STAMP_ARG
#define STAMP_PASS
namespace k1 {
#define USE1(x)
constexpr int BM = 160, A_BYTES = BM * 128, B_BYTES = 64 * 256, NKS = 10;
constexpr int L_A = 0, L_B = 4 * A_BYTES, L_ST = L_B + 4 * B_BYTES, LDS_BYTES = L_ST + 4096;
enum { T_Q = 0, T_K = 1, T_V = 2, T_Z1 = 3, T_Z2 = 4 };
struct Params { const u16* x; const u16* wq; const u16* wk; const u16* wv; const u16* win; char* ws; };

struct Ld { rsrc_t ra, rb; unsigned va[2], vb[2], sa, sb, sbinc, da, db; const char* pk[4]; unsigned pinc[4]; };
template <bool KV, int S> DI void ld_issue(Ld& L) {
#pragma unroll
    for (int j = 0; j < 5; ++j) bdma(L.ra, L.va[j & 1], L.sa + S * 128 + j * 8 * 1280, L.da + (S & 3) * A_BYTES + j * 1024);
#pragma unroll
    for (int j = 0; j < 4; ++j) {
        if constexpr (KV) { glds(L.pk[j], L.db + (S & 3) * B_BYTES + j * 1024); L.pk[j] += L.pinc[j]; asm volatile("" : "+v"(L.pk[j])); }
        else bdma(L.rb, L.vb[(j >> 1) & 1], L.sb + S * L.sbinc + j * (L.sbinc >> 4), L.db + (S & 3) * B_BYTES + j * 1024);
    }
}
template <bool KV, int S> DI void ld_step(Ld& L) {
    if constexpr (S + 2 < NKS) { ld_issue<KV, S + 2>(L); WAIT_BAR(9); } else WAIT_BAR(0);
    if constexpr (S + 2 < NKS) ld_step<KV, S + 1>(L);
}
template <bool KV> DI void loader(Ld& L STAMP_ARG) {
    ld_issue<KV, 0>(L); ld_issue<KV, 1>(L);
    STAMP(2);
    WAIT_BAR(9);
    STAMP(3);
    ld_step<KV, 0>(L);
}

struct Frag { u32x4 x[5], w[2]; };
template <int S, int KS> DI void rd(Frag& f, lds_ptr lds, const unsigned (&xo)[5], unsigned wo0, unsigned wo1) {
    constexpr int sa = (S & 3) * A_BYTES, sb = (S & 3) * B_BYTES + KS * 8192;
#pragma unroll
    for (int n = 0; n < 2; ++n) {
        lds_ptr wp = lds + (n ? wo1 : wo0) + sb;
        const u32x2 lo = ldstr(wp), hi = ldstr(wp + 1024);
        f.w[n] = u32x4{lo[0], lo[1], hi[0], hi[1]};
    }
#pragma unroll
    for (int m = 0; m < 5; ++m) f.x[m] = ldsr128(lds + (xo[m] ^ (KS * 64)) + sa);
}
struct Stat { f32x4 s0, q0, s1, q1, cs; };
template <int S, int KS, int I> DI void rd1(Frag& f, lds_ptr lds, const unsigned (&xo)[5], unsigned wo0, unsigned wo1) {
    constexpr int sa = (S & 3) * A_BYTES, sb = (S & 3) * B_BYTES + KS * 8192;
    if constexpr (I < 4) {
        const u32x2 t = ldstr(lds + ((I >> 1) ? wo1 : wo0) + sb + (I & 1) * 1024);
        f.w[I >> 1][2 * (I & 1)] = t[0]; f.w[I >> 1][2 * (I & 1) + 1] = t[1];
    } else f.x[I - 4] = ldsr128(lds + (xo[I - 4] ^ (KS * 64)) + sa);
}
template <bool FT, int KSG, bool RD, int S, int KS>
DI void mm(const Frag& fc, Frag& fn, lds_ptr lds, const unsigned (&xo)[5], unsigned wo0, unsigned wo1, f32x4 (&acc)[5][2], int wc, Stat& st, const u32x4& ones) {
#define K1_MM(i) do { if (FT) mfma16_acc(acc[(i) >> 1][(i) & 1], fc.w[(i) & 1], fc.x[(i) >> 1]); else mfma16_acc(acc[(i) >> 1][(i) & 1], fc.x[(i) >> 1], fc.w[(i) & 1]); } while (0)
#define K1_RD(i) do { if constexpr (RD) rd1<S, KS, i>(fn, lds, xo, wo0, wo1); SBAR(); } while (0)
    K1_MM(0); K1_RD(0); K1_MM(1); K1_RD(1); K1_MM(2); K1_RD(2); K1_MM(3); K1_RD(3); K1_MM(4); K1_RD(4);
    K1_MM(5); K1_RD(5); K1_MM(6); K1_RD(6); K1_MM(7); K1_RD(7); K1_MM(8); K1_RD(8); K1_MM(9);
#undef K1_MM
#undef K1_RD
    mfma16_acc(st.s0, fc.x[0], ones); mfma16_acc(st.q0, fc.x[0], fc.x[0]); mfma16_acc(st.cs, ones, fc.w[0]);
    if ((KSG & 3) == wc) { mfma16_acc(st.s1, fc.x[4], ones); mfma16_acc(st.q1, fc.x[4], fc.x[4]); }
    SBAR();
}
template <bool FT, int S>
DI void stage(lds_ptr lds, Frag& f0, Frag& f1, const unsigned (&xo)[5], unsigned wo0, unsigned wo1, int wc,
              f32x4 (&acc)[5][2], Stat& st, const u32x4& ones STAMP_ARG) {
    if constexpr (S + 1 < NKS) BAR_LDS();
    STAMP(3 + S);
    mm<FT, 2 * S, true, S, 1>(f0, f1, lds, xo, wo0, wo1, acc, wc, st, ones);
    mm<FT, 2 * S + 1, (S + 1 < NKS), S + 1, 0>(f1, f0, lds, xo, wo0, wo1, acc, wc, st, ones);
    if constexpr (S + 1 < NKS) stage<FT, S + 1>(lds, f0, f1, xo, wo0, wo1, wc, acc, st, ones STAMP_PASS);
}
template <bool FT>
DI void mainloop(lds_ptr lds, int lane, int wr, int wc, f32x4 (&acc)[5][2], Stat& st STAMP_ARG) {
    const int r15 = lane & 15, g = lane >> 4;
    const unsigned xb = L_A + (80 * wr + r15) * 128 + ((g ^ ((r15 >> 1) & 7)) << 4);
    unsigned xo[5];
#pragma unroll
    for (int m = 0; m < 4; ++m) xo[m] = xb + (m ^ wc) * 2048;
    xo[4] = xb + 8192;
    const int q = r15 >> 2, p = lane & 3;
    const int fk = (q << 1) | ((g & 1) << 3);
    const unsigned wo0 = (L_B + (8 * g + q) * 256 + (((4 * wc + (p >> 1)) ^ fk) << 4) + 8 * (p & 1)) ^ (wr * 32), wo1 = wo0 ^ 32;
    Frag f0, f1;
    u32x4 ones = u32x4{0x3F803F80u, 0x3F803F80u, 0x3F803F80u, 0x3F803F80u};
    asm volatile("" : "+v"(ones), "+v"(st.s0), "+v"(st.q0), "+v"(st.s1), "+v"(st.q1), "+v"(st.cs));
#pragma unroll
    for (int m = 0; m < 5; ++m) asm volatile("" : "+v"(acc[m][0]), "+v"(acc[m][1]));
    BAR_LDS();
    rd<0, 0>(f0, lds, xo, wo0, wo1);
    stage<FT, 0>(lds, f0, f1, xo, wo0, wo1, wc, acc, st, ones STAMP_PASS);
    asm volatile("s_nop 7\n\ts_nop 7" ::: "memory");
}
DI void gelu40(float (&v)[40]) {
    float t[40];
#pragma unroll
    for (int i = 0; i < 40; ++i) t[i] = v[i] * __builtin_fmaf(v[i] * v[i], -0.1029432397f, -2.302208198f);
    SBAR();
#pragma unroll
    for (int i = 0; i < 40; ++i) t[i] = __builtin_amdgcn_exp2f(t[i]);
    SBAR();
#pragma unroll
    for (int i = 0; i < 40; ++i) t[i] = 1.f + t[i];
    SBAR();
#pragma unroll
    for (int i = 0; i < 40; ++i) t[i] = __builtin_amdgcn_rcpf(t[i]);
    SBAR();
#pragma unroll
    for (int i = 0; i < 40; ++i) v[i] *= t[i];
    SBAR();
}
template <int TYPE>
DI void epilogue(char* ws, const f32x4 (&acc)[5][2], const LDSP float* csum, const LDSP float* ra, const LDSP float* rbv, int tw, int col0, int lane, int wr, int wc STAMP_ARG) {
    const int r15 = lane & 15, g = lane >> 4;
    constexpr bool FT = (TYPE == T_Q || TYPE == T_K || TYPE == T_Z1);
    float cf[2][4];
#pragma unroll
    for (int n = 0; n < 2; ++n) {
        if constexpr (FT) { const f32x4 c4 = *(const LDSP f32x4*)(csum + 32 * wc + 16 * (n ^ wr) + 4 * g); for (int j = 0; j < 4; ++j) cf[n][j] = c4[j]; }
        else cf[n][0] = csum[32 * wc + 16 * (n ^ wr) + r15];
    }
    f32x4 av[5], bv[5];
#pragma unroll
    for (int mp = 0; mp < 5; ++mp) {
        const int m = mp < 4 ? (mp ^ wc) : 4;
        if constexpr (FT) { av[mp][0] = ra[80 * wr + 16 * m + r15]; bv[mp][0] = rbv[80 * wr + 16 * m + r15]; }
        else { av[mp] = *(const LDSP f32x4*)(ra + 80 * wr + 16 * m + 4 * g); bv[mp] = *(const LDSP f32x4*)(rbv + 80 * wr + 16 * m + 4 * g); }
    }
    USE1(av[4][0]); USE1(bv[4][0]); STAMP(16);
    const rsrc_t rws = mk_rsrc(ws, (unsigned)OFF_Z2N);
    float v[40];
#pragma unroll
    for (int mp = 0; mp < 5; ++mp)
#pragma unroll
        for (int n = 0; n < 2; ++n)
#pragma unroll
            for (int j = 0; j < 4; ++j) {
                constexpr float sc = (TYPE == T_Q) ? 0.32259534218f : 1.f;
                if constexpr (FT) v[8 * mp + 4 * n + j] = __builtin_fmaf(av[mp][0] * sc, acc[mp][n][j], (bv[mp][0] * sc) * cf[n][j]);
                else v[8 * mp + 4 * n + j] = __builtin_fmaf(av[mp][j], acc[mp][n][j], bv[mp][j] * cf[n][0]);
            }
    USE1(v[39]); STAMP(17);
    if constexpr (TYPE == T_Z1 || TYPE == T_Z2) { SBAR(); gelu40(v); }
    unsigned pk[5][4];
#pragma unroll
    for (int mp = 0; mp < 5; ++mp) {
        const float* vv = v + 8 * mp;
        const unsigned pa0 = pk2(vv[0], vv[1]), pa1 = pk2(vv[2], vv[3]), pb0 = pk2(vv[4], vv[5]), pb1 = pk2(vv[6], vv[7]);
        pk[mp][0] = wr ? pb0 : pa0; pk[mp][1] = wr ? pb1 : pa1; pk[mp][2] = wr ? pa0 : pb0; pk[mp][3] = wr ? pa1 : pb1;
        if constexpr (TYPE != T_K && TYPE != T_V) {
            auto x0 = __builtin_amdgcn_permlane16_swap(pk[mp][0], pk[mp][2], false, false);
            auto x1 = __builtin_amdgcn_permlane16_swap(pk[mp][1], pk[mp][3], false, false);
            pk[mp][0] = x0[0]; pk[mp][1] = x1[0]; pk[mp][2] = x0[1]; pk[mp][3] = x1[1];
        }
        asm volatile("" : "+v"(pk[mp][0]), "+v"(pk[mp][1]), "+v"(pk[mp][2]), "+v"(pk[mp][3]));
    }
    STAMP(18);
#pragma unroll
    for (int mp = 0; mp < 5; ++mp) {
        const int m = mp < 4 ? (mp ^ wc) : 4;
        const int tb = tw + 16 * m;
        const unsigned t00 = pk[mp][0], t01 = pk[mp][1], t10 = pk[mp][2], t11 = pk[mp][3];
        if (tb < S_) {
            if constexpr (TYPE == T_K) {
                const int t = tb + r15;
                const unsigned kt = (unsigned)OFF_KF + (t >> 5) * KT_BYTES + (t & 31) * 16;
                st64(rws, kt + (g >> 1) * 512 + (g & 1) * 8, u32x2{t00, t01});
                if (g == 1) st64(rws, kt + 1024, u32x2{t10, t11});
                if (g == 2) st64(rws, kt + 1024 + 8, u32x2{0x00003F80u, 0u});
            } else if constexpr (TYPE == T_V) {
                const unsigned vt = (unsigned)OFF_VF + (tb >> 5) * VT_BYTES + ((((tb >> 4) & 1) * 2 + (g & 1)) * V_SLOTS) * 16 + (g >> 1) * 8;
                st64(rws, vt + r15 * 16, u32x2{t00, t01});
                if (r15 >= 4 && r15 < 8) st64(rws, vt + (12 + r15) * 16, u32x2{t10, t11});
                if (r15 == 8) st64(rws, vt + 20 * 16, u32x2{0x3F803F80u, 0x3F803F80u});
                if (r15 == 9) st64(rws, vt + 21 * 16, u32x2{0u, 0u});
            } else {
                unsigned dst;
                if constexpr (TYPE == T_Z2) dst = (unsigned)OFF_Z2R + (unsigned)(((tb >> 3) + (g >> 1)) * FH_ + col0 + 16 * (g & 1) + r15) * 16;
                else dst = (unsigned)(TYPE == T_Q ? OFF_Q : OFF_Z1) + (unsigned)((tb + r15) * (TYPE == T_Q ? 1280 : 1728) + (col0 + 16 * (g & 1) + 8 * (g >> 1)) * 2);
                st128(rws, dst, u32x4{t00, t01, t10, t11});
            }
        }
    }
}

__global__ __launch_bounds__(768, 3) void k1_proj(Params P) {
    extern __shared__ __attribute__((aligned(16))) char smem[];
    const int tid = threadIdx.x, lane = tid & 63, w = __builtin_amdgcn_readfirstlane(tid >> 6);
    STAMP_DECL; STAMP(0); RSTAMP(30);
    const int b = blockIdx.x, j0 = b >> 3, n0 = (b & 7) * 31 + j0;
    if (j0 == 31 || n0 >= 247) {
        if (b == 255 && tid < 64) ((u32x4*)(P.ws + OFF_FLAG))[tid * 8] = u32x4{0u, 0u, 0u, 0u};
        return;
    }
    int rb, cb;
    if (n0 < 133) { cb = n0 / 7; rb = n0 - cb * 7; } else { const int m = n0 - 133; cb = m / 6; rb = 7 + m - cb * 6; }
    const int row0 = rb * BM;
    lds_ptr lds = (lds_ptr)smem;
    const unsigned lds0 = (unsigned)(uintptr_t)smem;
    if (w >= 8) {
        const int L = w - 8;
        Ld ld;
        ld.ra = mk_rsrc(P.x, S_ * 1280);
        const int r8 = lane >> 3, c8 = lane & 7, k4 = lane >> 4, c16 = lane & 15;
#pragma unroll
        for (int i = 0; i < 2; ++i) ld.va[i] = r8 * 1280 + ((c8 ^ ((4 * ((L + i) & 1) + k4) & 7)) << 4);
        ld.sa = (row0 + 40 * L) * 1280;
        ld.da = lds0 + L_A + 5 * L * 1024; ld.db = lds0 + L_B + 4 * L * 1024;
        const unsigned ldw = cb < 5 ? 1280u : 3456u;
        ld.rb = cb < 5 ? mk_rsrc(P.wq, D_ * 1280) : mk_rsrc(P.win, D_ * 3456);
#pragma unroll
        for (int i = 0; i < 2; ++i) ld.vb[i] = k4 * ldw + ((c16 ^ (k4 << 1) ^ (i << 3)) << 4);
        ld.sb = (cb < 5 ? cb : cb - 5) * 256 + 16 * L * ldw; ld.sbinc = 64 * ldw;
        STAMP(1);
        if (cb != 18) loader<false>(ld STAMP_PASS);
        else {
#pragma unroll
            for (int j = 0; j < 4; ++j) {
                const int k = 16 * L + 4 * j + k4, c = c16 ^ (k4 << 1) ^ (((j >> 1) & 1) << 3), cc = c & 3;
                if (c < 8) { ld.pk[j] = (const char*)P.win + k * 3456 + 13 * 256 + c * 16; ld.pinc[j] = 64 * 3456; }
                else { ld.pk[j] = (const char*)(c < 12 ? P.wk : P.wv) + k * 40 + (cc == 1 ? 16 : cc == 2 ? 24 : 0); ld.pinc[j] = 64 * 40; }
            }
            loader<true>(ld STAMP_PASS);
        }
        return;
    }
    const int wr = w & 1, wc = w >> 1;
    int type, col0;
    if (cb < 5) { type = T_Q; col0 = cb * 128 + wc * 32; }
    else { const int zc = (cb - 5) * 128 + wc * 32;
           if (cb == 18 && wc >= 2) { type = (wc == 2) ? T_K : T_V; col0 = 0; }
           else if (zc < FH_) { type = T_Z1; col0 = zc; } else { type = T_Z2; col0 = zc - FH_; } }
    const bool ft = (type == T_Q || type == T_K || type == T_Z1);
    f32x4 acc[5][2];
#pragma unroll
    for (int m = 0; m < 5; ++m) { acc[m][0] = f32x4{0, 0, 0, 0}; acc[m][1] = f32x4{0, 0, 0, 0}; }
    Stat st; st.s0 = st.q0 = st.s1 = st.q1 = st.cs = f32x4{0, 0, 0, 0};
    STAMP(2);
    if (ft) mainloop<true>(lds, lane, wr, wc, acc, st STAMP_PASS);
    else mainloop<false>(lds, lane, wr, wc, acc, st STAMP_PASS);
    STAMP(13);
    const int r15 = lane & 15, g = lane >> 4;
    LDSP float* rsum = (LDSP float*)(lds + L_ST); LDSP float* rsq = rsum + 160; LDSP float* csum = rsum + 320; LDSP float* ra = rsum + 448; LDSP float* rbv = rsum + 608;
    LDSP float* p4s = rsum + 768; LDSP float* p4q = rsum + 896;
    if (r15 == 0) { *(LDSP f32x4*)(rsum + 80 * wr + 16 * wc + 4 * g) = st.s0; *(LDSP f32x4*)(p4s + wc * 32 + wr * 16 + 4 * g) = st.s1; }
    if ((r15 >> 2) == g) {
        const int j = r15 & 3;
        rsq[80 * wr + 16 * wc + r15] = j == 0 ? st.q0[0] : j == 1 ? st.q0[1] : j == 2 ? st.q0[2] : st.q0[3];
        p4q[wc * 32 + wr * 16 + r15] = j == 0 ? st.q1[0] : j == 1 ? st.q1[1] : j == 2 ? st.q1[2] : st.q1[3];
    }
    if (g == 0) csum[32 * wc + 16 * wr + r15] = st.cs[0];
    STAMP(19);
    BAR_LDS();
    STAMP(20);
    if (tid < BM) {
        const int tr = tid >= 80, tl = tid - 80 * tr, o = tr * 16 + (tl & 15);
        float sm = rsum[tid], sq = rsq[tid];
        const float s4 = p4s[o] + p4s[32 + o] + p4s[64 + o] + p4s[96 + o], q4 = p4q[o] + p4q[32 + o] + p4q[64 + o] + p4q[96 + o];
        if (tl >= 64) { sm = s4; sq = q4; }
        const float mu = sm * (1.f / D_), var = fmaxf(sq * (1.f / D_) - mu * mu, 0.f);
        const float rs = __builtin_amdgcn_rsqf(var + 1e-5f);
        ra[tid] = rs; rbv[tid] = -mu * rs;
    }
    BAR_LDS();
    STAMP(14);
    const int tw = row0 + 80 * wr;
    switch (type) {
        case T_Q: epilogue<T_Q>(P.ws, acc, csum, ra, rbv, tw, col0, lane, wr, wc STAMP_PASS); break;
        case T_K: epilogue<T_K>(P.ws, acc, csum, ra, rbv, tw, col0, lane, wr, wc STAMP_PASS); break;
        case T_V: epilogue<T_V>(P.ws, acc, csum, ra, rbv, tw, col0, lane, wr, wc STAMP_PASS); break;
        case T_Z1: epilogue<T_Z1>(P.ws, acc, csum, ra, rbv, tw, col0, lane, wr, wc STAMP_PASS); break;
        default: epilogue<T_Z2>(P.ws, acc, csum, ra, rbv, tw, col0, lane, wr, wc STAMP_PASS); break;
    }
    STAMP(15); RSTAMP(31); STAMP_FLUSH_K(0);
}
}
namespace k2 {
constexpr int KV_HALF = 12288, KV_STAGE = 2 * KV_HALF, W_STAGE = 32768;
constexpr int L_KV = 0, L_W = 3 * KV_STAGE, L_RED = L_W + 2 * W_STAGE, L_DUMMY = L_RED + 1024, LDS_BYTES = L_DUMMY + 6144;
enum { C_KV0 = 64, C_KV12, C_GAT, C_NORM, C_PUB, C_FLAG, C_ATT = 72  , C_PAIR = 80, C_PROG = 88   };
DI void lds_add(lds_ptr lds, int word) {
    asm volatile("s_mov_b64 exec, 1\n\tds_add_u32 %0, %1\n\ts_mov_b64 exec, -1" :: "v"((unsigned)(L_RED + 4 * word) + (unsigned)(uintptr_t)lds), "v"(1u) : "memory");
}
DI void lds_wait_ge(lds_ptr lds, int word, unsigned target) {
    const volatile LDSP unsigned* p = (const volatile LDSP unsigned*)(lds + L_RED + 4 * word);
    for (unsigned spin = 0; spin < (1u << 26); ++spin) {
        if ((unsigned)__builtin_amdgcn_readfirstlane(*p) >= target) break;
        __builtin_amdgcn_s_sleep(1);
    }
    asm volatile("" ::: "memory");
}
constexpr float M_SHIFT = 100.f;
constexpr int ZSTEP = 2 * FH_ * 16;
struct Params { const u16* wsp; const u16* bsp; const u16* wo; const u16* wout; char* ws; };
#define USE(x) asm volatile("" :: "v"(x))
#define EXP2(x) __builtin_amdgcn_exp2f(x)
DI float rowmax32(const f32x16& s) {
    float a = fmaxf(fmaxf(s[0], s[1]), s[2]), b = fmaxf(fmaxf(s[3], s[4]), s[5]);
#pragma unroll
    for (int r = 6; r < 14; r += 4) { a = fmaxf(fmaxf(a, s[r]), s[r + 1]); b = fmaxf(fmaxf(b, s[r + 2]), s[r + 3]); }
    a = fmaxf(fmaxf(a, s[14]), s[15]); a = fmaxf(a, b);
    auto rr = __builtin_amdgcn_permlane32_swap(__float_as_uint(a), __float_as_uint(a), false, false);
    return fmaxf(__uint_as_float(rr[0]), __uint_as_float(rr[1]));
}
DI void pack16(const f32x16& s, u32x4& p0, u32x4& p1) {
    p0 = u32x4{pk2(s[0], s[1]), pk2(s[2], s[3]), pk2(s[4], s[5]), pk2(s[6], s[7])};
    p1 = u32x4{pk2(s[8], s[9]), pk2(s[10], s[11]), pk2(s[12], s[13]), pk2(s[14], s[15])};
}
DI f32x16 attn_fallback(const char* ws, u32x4 q0, u32x2 q1, int lane) {
    const int r = lane & 31, h = lane >> 5;
    if (h) q1 = u32x2{0u, 0u};
    f32x16 o = {}; float m = -INFINITY;
#pragma unroll 1
    for (int T = 0; T < 64; ++T) {
        const char* kt = ws + OFF_KF + T * KT_BYTES; const char* vt = ws + OFF_VF + T * VT_BYTES;
        const u32x4 k0 = *(const u32x4*)(kt + h * 512 + r * 16); const u32x2 k1 = *(const u32x2*)(kt + 1024 + r * 16 + h * 8);
        const int rv = min(r, 21);
        const u32x4 v0 = *(const u32x4*)(vt + (h * V_SLOTS + rv) * 16), v1 = *(const u32x4*)(vt + ((2 + h) * V_SLOTS + rv) * 16);
        f32x16 s = mfma32(k0, q0, f32x16{}); s = mfma32k8(k1, q1, s);
        const float mn = fmaxf(m, rowmax32(s));
        const float al = (mn == -INFINITY) ? 1.f : EXP2(m - mn);
        const float ms = (mn == -INFINITY) ? 0.f : mn;
#pragma unroll
        for (int i = 0; i < 16; ++i) { s[i] = EXP2(s[i] - ms); o[i] *= al; }
        m = mn;
        u32x4 p0, p1; pack16(s, p0, p1);
        o = mfma32(v0, p0, o); o = mfma32(v1, p1, o);
    }
    return o;
}

struct St {
    u32x4 qa0, qb0; u32x2 qa1, qb1;
    f32x16 sa, sb, oa, ob;
    u32x4 pa0, pa1, pb0, pb1;
    u32x4 k0[2]; u32x2 k1[2]; u32x4 v0[2], v1[2];
};
#define K2_SCHED 4
#define E_(S, i) S[i] = EXP2(S[i]); USE(S[i])
#define C_(P, j, S, i) P[j] = pk2(S[i], S[i + 1]); USE(P[j])
#define K2_STEP(S, P0, P1, SP, PP1, M0, M1, M2, M3, R0, R1, R2)                                          \
    do {                                                                                                 \
        C_(PP1, 3, SP, 14); M0; R0; SBAR(); E_(S, 0); E_(S, 1); E_(S, 2); E_(S, 3); SBAR();              \
        M1; R1; C_(P0, 0, S, 0); E_(S, 4); E_(S, 5); C_(P0, 1, S, 2); E_(S, 6); E_(S, 7); SBAR();        \
        M2; R2; C_(P0, 2, S, 4); E_(S, 8); E_(S, 9); C_(P0, 3, S, 6); E_(S, 10); E_(S, 11); SBAR();      \
        M3; C_(P1, 0, S, 8); E_(S, 12); E_(S, 13); C_(P1, 1, S, 10); E_(S, 14); E_(S, 15); C_(P1, 2, S, 12); SBAR(); \
    } while (0)
#define K2_FLIP 1
#define K2_PLO 1
#define K2_PHI 2
#define K2_ALIGN 1
template <int STEP> DI void prio_step() {
    if constexpr (STEP == 0) __builtin_amdgcn_s_setprio(3);
    if constexpr (STEP == 3) __builtin_amdgcn_s_setprio(2);
    if constexpr (STEP == 6) __builtin_amdgcn_s_setprio(1);
}
struct Pr { volatile LDSP unsigned* mine; const volatile LDSP unsigned* other; unsigned it, seen; };
DI void prio_rel(Pr& p) {
}
template <int I>
DI void att_iter(St& s, Pr& pr, lds_ptr pk0, lds_ptr pk1, lds_ptr pv) {
    constexpr int c = I & 1, n = c ^ 1;
    prio_rel(pr);
    prio_step<2 * I>();
    K2_STEP(s.sa, s.pa0, s.pa1, s.sb, s.pb1,
            s.sb = mfma32(s.k0[c], s.qb0, f32x16{}),
            s.sb = mfma32k8(s.k1[c], s.qb1, s.sb),
            s.ob = mfma32(s.v0[n], s.pb0, s.ob),
            s.ob = mfma32(s.v1[n], s.pb1, s.ob),
            { s.k0[n] = ldsr128(pk0 + I * KT_BYTES); s.k1[n] = *(const LDSP u32x2*)(pk1 + I * KT_BYTES); },
            { s.v0[c] = ldsr128(pv + I * VT_BYTES); },
            { s.v1[c] = ldsr128(pv + I * VT_BYTES + 2 * V_SLOTS * 16); });
    prio_step<2 * I + 1>();
    K2_STEP(s.sb, s.pb0, s.pb1, s.sa, s.pa1,
            s.sa = mfma32(s.k0[n], s.qa0, f32x16{}),
            s.sa = mfma32k8(s.k1[n], s.qa1, s.sa),
            s.oa = mfma32(s.v0[c], s.pa0, s.oa),
            s.oa = mfma32(s.v1[c], s.pa1, s.oa),
            {}, {}, {});
}
DI void att_loop(St& s, lds_ptr lds, lds_ptr kvb, unsigned k0o, unsigned k1o, unsigned vo, int w STAMP_ARG) {
    int buf = 0;
    Pr pr; pr.mine = (volatile LDSP unsigned*)(lds + L_RED) + C_PROG + w; pr.other = (const volatile LDSP unsigned*)(lds + L_RED) + C_PROG + (w ^ 4); pr.it = 0; pr.seen = 0;
    const volatile LDSP unsigned* cw = (const volatile LDSP unsigned*)(lds + L_RED);
    unsigned seen = 0;
#pragma unroll 1
    for (int ss = 0; ss < 8; ++ss) {
        const int word = ss == 0 ? C_KV0 : ss == 1 ? C_KV12 : C_GAT;
        const unsigned target = ss == 1 ? 8u : ss < 3 ? 0u : 4u * (ss - 1);
        if ((unsigned)__builtin_amdgcn_readfirstlane(seen) < target) lds_wait_ge(lds, word, target);
        if (ss > 0) lds_wait_ge(lds, C_ATT + ss - 1, 8u);
        lds_ptr kv = kvb + buf * KV_STAGE;
        att_iter<0>(s, pr, kv + k0o, kv + k1o, kv + vo);
        if (ss == 0) { WAIT_VM(0); lds_add(lds, C_KV12); }
        att_iter<1>(s, pr, kv + k0o, kv + k1o, kv + vo);
        att_iter<2>(s, pr, kv + k0o, kv + k1o, kv + vo);
        seen = cw[ss == 0 ? C_KV12 : C_GAT];
        att_iter<3>(s, pr, kv + k0o, kv + k1o, kv + vo);
        asm volatile("s_waitcnt lgkmcnt(0)" ::: "memory");
        lds_add(lds, C_ATT + ss);
        buf = (buf == 2) ? 0 : buf + 1;
    }
}
DI void attention(const Params& P, lds_ptr lds, unsigned lds0, int b, int w, int lane STAMP_ARG) {
    const int p = w & 3, hf = w >> 2, r31 = lane & 31, h = lane >> 5;
    char* ws = P.ws;
    St s;
    __builtin_amdgcn_s_setprio(2);
    BAR_LDS();
    const rsrc_t rkv = mk_rsrc(ws, (unsigned)OFF_Z1);
    unsigned skv[3], kinc[3];
#pragma unroll
    for (int i = 0; i < 3; ++i) {
        const int pc = 3 * w + i, ph = pc >= 12, j = pc - ph * 12;
        skv[i] = j < 6 ? (unsigned)OFF_KF + (ph * 32 + 1) * KT_BYTES + j * 1024 : (unsigned)OFF_VF + (ph * 32) * VT_BYTES + (j - 6) * 1024;
        kinc[i] = j < 6 ? 4 * KT_BYTES : 4 * VT_BYTES;
    }
    const unsigned kvdst = lds0 + L_KV + 3 * w * 1024;
    STAMP(18);
#pragma unroll
    for (int i = 0; i < 3; ++i) bdma(rkv, lane * 16, skv[i], kvdst + i * 1024);
#pragma unroll
    for (int i = 0; i < 2; ++i) {
        const int pc = min(2 * w + i, 13);
        const unsigned src = pc < 10 ? (unsigned)OFF_Q + (8 * b) * 1280 + pc * 1024 : (unsigned)OFF_KF + (pc >= 12 ? 32 * KT_BYTES : 0) + (pc & 1) * 1024;
        bdma(rkv, lane * 16, src, lds0 + L_W + W_STAGE + pc * 1024);
    }
    STAMP(19);
    WAIT_VM(0);
    lds_add(lds, C_KV0);
#pragma unroll
    for (int st = 1; st < 3; ++st)
#pragma unroll
        for (int i = 0; i < 3; ++i) bdma(rkv, lane * 16, skv[i] + st * kinc[i], kvdst + st * KV_STAGE + i * 1024);
    STAMP(20);
    lds_wait_ge(lds, C_KV0, 8u);
    STAMP(21);
    const int tA = 8 * b + 2 * p;
    {
        lds_ptr qp = lds + L_W + W_STAGE + (2 * p) * 1280 + r31 * 40;
        auto rd16 = [&](lds_ptr a) { const u32x2 lo = *(const LDSP u32x2*)a, hi = *(const LDSP u32x2*)(a + 8); return u32x4{lo[0], lo[1], hi[0], hi[1]}; };
        s.qa0 = rd16(qp + h * 16); s.qb0 = rd16(qp + 1280 + h * 16);
        s.qa1 = *(const LDSP u32x2*)(qp + 32); s.qb1 = *(const LDSP u32x2*)(qp + 1280 + 32);
        lds_ptr k0p = lds + L_W + W_STAGE + 10240;
        const u32x4 k00 = ldsr128(k0p + h * 512 + r31 * 16); const u32x2 k01 = *(const LDSP u32x2*)(k0p + 1024 + r31 * 16 + h * 8);
        lds_ptr kfp = k0p + hf * 2048;
        s.k0[0] = ldsr128(kfp + h * 512 + r31 * 16); s.k1[0] = *(const LDSP u32x2*)(kfp + 1024 + r31 * 16 + h * 8);
        if (h) { s.qa1 = u32x2{0u, 0u}; s.qb1 = u32x2{0u, 0u}; }
        f32x16 a = mfma32(k00, s.qa0, f32x16{}), c = mfma32(k00, s.qb0, f32x16{});
        a = mfma32k8(k01, s.qa1, a); c = mfma32k8(k01, s.qb1, c);
        const float ma = rowmax32(a) + M_SHIFT, mb = rowmax32(c) + M_SHIFT;
        if (h) { s.qa1[0] = pk2(-ma, 0.f); s.qb1[0] = pk2(-mb, 0.f); }
    }
    s.sa = mfma32(s.k0[0], s.qa0, f32x16{}); s.sa = mfma32k8(s.k1[0], s.qa1, s.sa);
    s.oa = f32x16{}; s.ob = f32x16{}; s.sb = f32x16{};
    s.pb0 = u32x4{0, 0, 0, 0}; s.pb1 = u32x4{0, 0, 0, 0}; s.v0[1] = u32x4{0, 0, 0, 0}; s.v1[1] = u32x4{0, 0, 0, 0};
    lds_ptr kvb = lds + L_KV + hf * KV_HALF;
    const unsigned k0o = h * 512 + r31 * 16, k1o = 1024 + r31 * 16 + h * 8, vo = 6144 + (h * V_SLOTS + min(r31, 21)) * 16;
    STAMP(3);
    att_loop(s, lds, kvb, k0o, k1o, vo, w STAMP_PASS);
    STAMP(15);
    s.pb1[3] = pk2(s.sb[14], s.sb[15]);
    s.ob = mfma32(s.v0[1], s.pb0, s.ob); s.ob = mfma32(s.v1[1], s.pb1, s.ob);
    __builtin_amdgcn_s_setprio(3);
    lds_wait_ge(lds, C_ATT + 5, 8u);
    LDSP f32x4* xs = (LDSP f32x4*)(lds + L_KV + 2 * KV_STAGE) + w * 3 * 64;
    LDSP f32x4* xr = (LDSP f32x4*)(lds + L_KV + 2 * KV_STAGE) + (w ^ 4) * 3 * 64;
    if (hf == 0) {
#pragma unroll
        for (int i = 0; i < 3; ++i) xs[i * 64 + lane] = f32x4{s.ob[4 * i], s.ob[4 * i + 1], s.ob[4 * i + 2], s.ob[4 * i + 3]};
    } else {
#pragma unroll
        for (int i = 0; i < 3; ++i) xs[i * 64 + lane] = f32x4{s.oa[4 * i], s.oa[4 * i + 1], s.oa[4 * i + 2], s.oa[4 * i + 3]};
        s.oa = s.ob; s.qa0 = s.qb0; s.qa1 = s.qb1;
    }
    asm volatile("s_waitcnt lgkmcnt(0)" ::: "memory");
    lds_add(lds, C_PAIR + w);
    lds_wait_ge(lds, C_PAIR + (w ^ 4), 1u);
    STAMP(16);
    f32x16 o = s.oa;
#pragma unroll
    for (int i = 0; i < 3; ++i) { const f32x4 t = xr[i * 64 + lane]; o[4 * i] += t[0]; o[4 * i + 1] += t[1]; o[4 * i + 2] += t[2]; o[4 * i + 3] += t[3]; }
    auto lr = __builtin_amdgcn_permlane32_swap(__float_as_uint(o[8]), __float_as_uint(o[8]), false, false);
    float l = __uint_as_float(lr[1]);
    bool ok = (l >= 0x1p-110f) && (l <= 0x1p100f);
#pragma unroll
    for (int i = 0; i < 12; ++i) ok = ok && (fabsf(o[i]) <= 0x1p126f || (h && i >= 8));
    if (__builtin_expect(!__all(ok), 0)) {
        o = attn_fallback(ws, s.qa0, s.qa1, lane);
        lr = __builtin_amdgcn_permlane32_swap(__float_as_uint(o[8]), __float_as_uint(o[8]), false, false);
        l = __uint_as_float(lr[1]);
    }
    const float rl = 1.f / l;
    char* op = ws + OFF_OU + (unsigned)((tA + hf) * 3072 + r31 * 40 + h * 8);
    *(u32x2*)(op) = u32x2{pk2(o[0] * rl, o[1] * rl), pk2(o[2] * rl, o[3] * rl)};
    *(u32x2*)(op + 16) = u32x2{pk2(o[4] * rl, o[5] * rl), pk2(o[6] * rl, o[7] * rl)};
    if (!h) *(u32x2*)(op + 32) = u32x2{pk2(o[8] * rl, o[9] * rl), pk2(o[10] * rl, o[11] * rl)};
}

DI void zload(u32x4& z, rsrc_t r, unsigned voff, unsigned soff) {
    asm volatile("buffer_load_dwordx4 %0, %1, %2, %3 offen sc1" : "+v"(z) : "v"(voff), "s"(r), "s"(soff) : "memory");
}
#define K2_GSPLIT 1
#define K2_PFSS 7
#define K2_POLL 16
#define GBURST_PRIO 2
#define GSTEP_PRIO 3
struct Gs {
    rsrc_t rkv, rw, rz; unsigned vkv, vw[8], skv[6], sw, dkv, dw, zoff;
    u32x4 z[16]; f32x16 g0, g1; u32x4 wa0, wa1;
};
template <int I> DI void kv_dma(const Gs& g) { bdma(g.rkv, g.vkv, g.skv[I], g.dkv + I * 1024); }
template <int I> DI void w_dma(const Gs& g) { bdma(g.rw, g.vw[I], g.sw + 2 * I * 4096, g.dw + I * 1024); }
DI void z_loads(Gs& g, unsigned zs) {
#pragma unroll
    for (int i = 0; i < 16; ++i) zload(g.z[i], g.rz, g.zoff, zs + i * ZSTEP);
}
template <int I> DI void kv_all(const Gs& g) { kv_dma<I>(g); if constexpr (I < 5) kv_all<I + 1>(g); }
template <int I> DI void w_all(const Gs& g) { w_dma<I>(g); if constexpr (I < 7) w_all<I + 1>(g); }
#define Z_TIE(g) "+v"(g.z[0]), "+v"(g.z[1]), "+v"(g.z[2]), "+v"(g.z[3]), "+v"(g.z[4]), "+v"(g.z[5]), "+v"(g.z[6]), "+v"(g.z[7]), \
                 "+v"(g.z[8]), "+v"(g.z[9]), "+v"(g.z[10]), "+v"(g.z[11]), "+v"(g.z[12]), "+v"(g.z[13]), "+v"(g.z[14]), "+v"(g.z[15])
template <int I> DI void g_burst(Gs& g, lds_ptr wp, unsigned wx) {
    u32x4 wn0, wn1;
    if constexpr (I == 0) { g.wa0 = ldsr128(wp + wx); g.wa1 = ldsr128(wp + wx + 16384); }
    if constexpr (I < 15) { wn0 = ldsr128(wp + (wx ^ ((I + 1) * 32))); wn1 = ldsr128(wp + (wx ^ ((I + 1) * 32)) + 16384); }
    g.g0 = mfma32(g.z[I], g.wa0, g.g0); g.g1 = mfma32(g.z[I], g.wa1, g.g1);
    if constexpr (I < 15) { g.wa0 = wn0; g.wa1 = wn1; }
    SBAR();
    if constexpr (I < 15) g_burst<I + 1>(g, wp, wx);
}
template <int J> DI void dma_piece(const Gs& g) { if constexpr (J < 8) w_dma<J>(g); else kv_dma<J - 8>(g); }
template <int N> DI void z_wait(u32x4& z) { asm volatile("s_waitcnt vmcnt(%1)" : "+v"(z) : "i"(N) : "memory"); }
template <int I, bool FIRST> DI void g_step(Gs& g, lds_ptr wp, unsigned wx, unsigned zs) {
    u32x4 wn0, wn1;
    if constexpr (I == 0) { g.wa0 = ldsr128(wp + wx); g.wa1 = ldsr128(wp + wx + 16384); }
    if constexpr (I < 15) { wn0 = ldsr128(wp + (wx ^ ((I + 1) * 32))); wn1 = ldsr128(wp + (wx ^ ((I + 1) * 32)) + 16384); }
    constexpr int nd = I < 3 ? 2 : I < 11 ? 1 : 0;
    constexpr int before = I <= 3 ? 2 * I : I <= 11 ? I + 3 : 14;
    z_wait<FIRST ? 15 + before : 29 - nd>(g.z[I]);
    g.g0 = mfma32(g.z[I], g.wa0, g.g0);
    SBAR();
    if constexpr (I < 3) { dma_piece<2 * I>(g); dma_piece<2 * I + 1>(g); } else if constexpr (I < 11) dma_piece<I + 3>(g);
    else __builtin_amdgcn_s_sleep(1);
    SBAR();
    g.g1 = mfma32(g.z[I], g.wa1, g.g1);
    zload(g.z[I], g.rz, g.zoff, zs + I * ZSTEP);
    if constexpr (I < 15) { g.wa0 = wn0; g.wa1 = wn1; }
    SBAR();
    if constexpr (I < 15) g_step<I + 1, FIRST>(g, wp, wx, zs);
}
template <bool HASG>
DI void gating(const Params& P, lds_ptr lds, unsigned lds0, int b, int gw, int lane, int t0, int f0 STAMP_ARG) {
    char* ws = P.ws;
    const int t256 = gw * 64 + lane, r31 = lane & 31, h = lane >> 5;
    __builtin_amdgcn_s_setprio(3);
    const char* zr0 = ws + OFF_Z2R + (unsigned)(b * FH_ * 16);
    u32x4 c[4];
#pragma unroll
    for (int i = 0; i < 4; ++i) c[i] = *(const u32x4*)(zr0 + (i < 3 || t256 < FH_ - 768 ? t256 + 256 * i : t256) * 16);
    if (gw == 0 && lane < 32) ((LDSP unsigned*)(lds + L_RED))[64 + lane] = 0u;
    BAR_LDS();
    STAMP(21);
    USE(c[0][0]); USE(c[1][0]); USE(c[2][0]); USE(c[3][0]);
    STAMP(22);
    if (t256 >= FH_ - 768) c[3] = u32x4{0, 0, 0, 0};
    LDSP float* red = (LDSP float*)(lds + L_RED);
    {
        float v[16];
#pragma unroll
        for (int j = 0; j < 8; ++j) { v[j] = 0.f; v[8 + j] = 0.f; }
#pragma unroll
        for (int i = 0; i < 4; ++i)
#pragma unroll
            for (int j = 0; j < 4; ++j) {
                const float lo = bflo(c[i][j]), hi = bfhi(c[i][j]);
                v[2 * j] += lo; v[2 * j + 1] += hi; v[8 + 2 * j] = __builtin_fmaf(lo, lo, v[8 + 2 * j]); v[8 + 2 * j + 1] = __builtin_fmaf(hi, hi, v[8 + 2 * j + 1]);
            }
        float u[8], t[4];
#pragma unroll
        for (int j = 0; j < 8; ++j) { auto r = __builtin_amdgcn_permlane32_swap(__float_as_uint(v[j]), __float_as_uint(v[j + 8]), false, false); u[j] = __uint_as_float(r[0]) + __uint_as_float(r[1]); }
#pragma unroll
        for (int j = 0; j < 4; ++j) { auto r = __builtin_amdgcn_permlane16_swap(__float_as_uint(u[j]), __float_as_uint(u[j + 4]), false, false); t[j] = __uint_as_float(r[0]) + __uint_as_float(r[1]); }
#pragma unroll
        for (int j = 0; j < 4; ++j) {
            t[j] += __uint_as_float(__builtin_amdgcn_update_dpp(0, __float_as_uint(t[j]), 0x128, 0xf, 0xf, false));
            t[j] += __uint_as_float(__builtin_amdgcn_update_dpp(0, __float_as_uint(t[j]), 0x124, 0xf, 0xf, false));
            t[j] += __uint_as_float(__builtin_amdgcn_update_dpp(0, __float_as_uint(t[j]), 0x122, 0xf, 0xf, false));
            t[j] += __uint_as_float(__builtin_amdgcn_update_dpp(0, __float_as_uint(t[j]), 0x121, 0xf, 0xf, false));
        }
        if ((lane & 15) == 0) { const int rho = lane >> 4; *(LDSP f32x4*)(red + gw * 16 + (rho >> 1) * 8 + (rho & 1) * 4) = f32x4{t[0], t[1], t[2], t[3]}; }
        asm volatile("s_waitcnt lgkmcnt(0)" ::: "memory");
        lds_add(lds, C_NORM);
        STAMP(2);
        lds_wait_ge(lds, C_NORM, 4u);
        float sm = 0, sq = 0;
#pragma unroll
        for (int j = 0; j < 4; ++j) { sm += red[j * 16 + (lane & 7)]; sq += red[j * 16 + 8 + (lane & 7)]; }
        const float mu = sm * (1.f / FH_), var = fmaxf(sq * (1.f / FH_) - mu * mu, 0.f), rs = __builtin_amdgcn_rsqf(var + 1e-5f), sh = -mu * rs;
        float a[8], bb[8];
#pragma unroll
        for (int j = 0; j < 8; ++j) { a[j] = __uint_as_float(__builtin_amdgcn_readlane(__float_as_uint(rs), j)); bb[j] = __uint_as_float(__builtin_amdgcn_readlane(__float_as_uint(sh), j)); }
        const rsrc_t zw = mk_rsrc(ws + OFF_Z2N + (unsigned)(b * FH_ * 16), FH_ * 16);
#pragma unroll
        for (int i = 0; i < 4; ++i) {
            u32x4 o;
#pragma unroll
            for (int j = 0; j < 4; ++j) o[j] = pk2(__builtin_fmaf(bflo(c[i][j]), a[2 * j], bb[2 * j]), __builtin_fmaf(bfhi(c[i][j]), a[2 * j + 1], bb[2 * j + 1]));
            __builtin_amdgcn_raw_buffer_store_b128(o, zw, (t256 + 256 * i) * 16, 0, 16);
        }
    }
    STAMP(23);
    Gs g;
    g.rkv = mk_rsrc(ws + OFF_KF, (unsigned)(OFF_Z1 - OFF_KF)); g.rw = mk_rsrc(P.wsp, S_ * 4096); g.rz = mk_rsrc(ws + OFF_Z2N, (unsigned)(S_ * FH_ * 2 + 4096));
    g.vkv = lane * 16;
#pragma unroll
    for (int i = 0; i < 8; ++i) g.vw[i] = (lane >> 5) * 4096 + (((lane & 31) ^ ((2 * i + (lane >> 5)) & 15)) << 4);
    g.sw = (t0 + 16 * gw) * 4096;
    const unsigned wdst = lds0 + L_W + 8 * gw * 1024;
    if (HASG) { g.dw = wdst; w_all<0>(g); g.sw += 512; asm volatile("s_waitcnt vmcnt(8)" ::: "memory"); }
    else WAIT_VM(0);
    unsigned* flags = (unsigned*)(ws + OFF_FLAG);
    if (lane == 0) {
        const unsigned old = __hip_atomic_fetch_add((LDSP unsigned*)(lds + L_RED) + C_PUB, 1u, __ATOMIC_RELAXED, __HIP_MEMORY_SCOPE_WORKGROUP);
        if (old == 3u) __hip_atomic_store(flags + (b >> 2) * 32 + (b & 3), 1u, __ATOMIC_RELAXED, __HIP_MEMORY_SCOPE_AGENT);
    }
    STAMP(5);
    const int ph = gw >> 1;
    const unsigned kvinc = (gw & 1) ? 4 * VT_BYTES : 4 * KT_BYTES;
    {
        const unsigned base = ((gw & 1) ? (unsigned)(OFF_VF - OFF_KF) + (ph * 32) * VT_BYTES : (ph * 32 + 1) * KT_BYTES) + 3 * kvinc;
#pragma unroll
        for (int i = 0; i < 6; ++i) g.skv[i] = base + i * 1024;
    }
    const unsigned kvdst = lds0 + L_KV + 6 * gw * 1024;
    if constexpr (!HASG) {
        lds_add(lds, C_GAT);
        int buf = 0;
#pragma unroll 1
        for (int m = 0; m < 5; ++m) {
            lds_wait_ge(lds, C_ATT + m, 8u);
            g.dkv = kvdst + buf * KV_STAGE;
            kv_all<0>(g);
#pragma unroll
            for (int i = 0; i < 6; ++i) g.skv[i] += kvinc;
            WAIT_VM(0);
            lds_add(lds, C_GAT);
            buf = (buf == 2) ? 0 : buf + 1;
        }
        lds_wait_ge(lds, C_ATT + K2_PFSS, 8u);
        {
            const int xg = b & 7, part = ((b >> 3) - 28) * 4 + gw;
            const unsigned colb = (xg >= 4 ? 640u : 0u) + (lane % 5) * 128u, rsub = lane / 5;
            const rsrc_t ro = mk_rsrc(P.wo, D_ * 1280), ru = mk_rsrc(P.wout, FH_ * 1280);
            if (part < 7) touch_lines8(ro, min(rsub, 11u) * 1280 + colb, (unsigned)(part * 96) * 1280, 12 * 1280);
            else touch_lines8(ru, min(rsub, 11u) * 1280 + colb, (unsigned)((part - 7) * 96) * 1280, 12 * 1280);
        }
        return;
    }
    g.zoff = (h * FH_ + f0 + r31) * 16;
    g.g0 = f32x16{}; g.g1 = f32x16{};
#pragma unroll
    for (int i = 0; i < 16; ++i) g.z[i] = u32x4{0, 0, 0, 0};
    lds_ptr wb = lds + L_W;
    const unsigned wx = r31 * 512 + (((lane & 15) ^ h) << 4);
    if (gw == 0) {
        const u32x4* fp = (const u32x4*)flags + lane * 8;
        for (unsigned spin = 0; spin < (1u << 22); ++spin) {
            __builtin_amdgcn_s_sleep(K2_POLL);
            u32x4 f;
            asm volatile("global_load_dwordx4 %0, %1, off sc1\n\ts_waitcnt vmcnt(0)" : "=&v"(f) : "v"(fp) : "memory");
            if (__all(f[0] == 1u && f[1] == 1u && f[2] == 1u && f[3] == 1u)) break;
        }
        lds_add(lds, C_FLAG);
    } else lds_wait_ge(lds, C_FLAG, 1u);
    STAMP(6);
    WAIT_VM(0);
    lds_add(lds, C_GAT);
    __builtin_amdgcn_s_setprio(GSTEP_PRIO);
    z_loads(g, 0);
    int buf = 0;
    auto stage_head = [&](int m) {
        lds_wait_ge(lds, C_GAT, 4u * (m + 1));
        if (m < 5) lds_wait_ge(lds, C_ATT + m, 8u);
        if (m == 5) g.rkv = mk_rsrc(ws, 0);
        if (m == 7) { g.rw = mk_rsrc(ws, 0); g.rz = mk_rsrc(ws, 0); }
        g.dkv = (m < 5) ? kvdst + buf * KV_STAGE : lds0 + L_DUMMY; g.dw = wdst + ((m + 1) & 1) * W_STAGE;
    };
    auto stage_tail = [&]() {
#pragma unroll
        for (int i = 0; i < 6; ++i) g.skv[i] += kvinc;
        g.sw += 512;
        asm volatile("s_waitcnt vmcnt(6) lgkmcnt(0)" ::: "memory");
        lds_add(lds, C_GAT);
        buf = (buf == 2) ? 0 : buf + 1;
    };
    stage_head(0); g_step<0, true>(g, wb, wx, 16 * ZSTEP); stage_tail();
#pragma unroll 1
    for (int m = 1; m < 8; ++m) { stage_head(m); g_step<0, false>(g, wb + (m & 1) * W_STAGE, wx, (m + 1) * 16 * ZSTEP); stage_tail(); }
    asm volatile("s_waitcnt vmcnt(0)" : Z_TIE(g) :: "memory");
    STAMP(15);
#pragma unroll
    for (int u = 0; u < 2; ++u) {
        const int t = t0 + 32 * u + r31;
        char* up = ws + OFF_OU + (unsigned)(t * 3072 + (640 + f0 + 4 * h) * 2);
        const f32x16& gg = u ? g.g1 : g.g0;
        u32x2 z1v[4]; float bias = 0.f;
        if (f0 < FH_) {
            const char* z1p = ws + OFF_Z1 + (unsigned)(t * 1728 + (f0 + 4 * h) * 2);
#pragma unroll
            for (int i = 0; i < 4; ++i) z1v[i] = *(const u32x2*)(z1p + i * 16);
            bias = bf2f(P.bsp[t]);
        }
#pragma unroll
        for (int i = 0; i < 4; ++i) {
            u32x2 o = u32x2{0u, 0u};
            if (f0 < FH_) {
                const u32x2 z1 = z1v[i];
                o = u32x2{pk2(bflo(z1[0]) * (gg[4 * i] + bias), bfhi(z1[0]) * (gg[4 * i + 1] + bias)),
                          pk2(bflo(z1[1]) * (gg[4 * i + 2] + bias), bfhi(z1[1]) * (gg[4 * i + 3] + bias))};
            }
            *(u32x2*)(up + i * 16) = o;
        }
    }
}

__global__ __launch_bounds__(768, 3) void k2_mix(Params P) {
    extern __shared__ __attribute__((aligned(16))) char smem[];
    const int lane = threadIdx.x & 63, w = __builtin_amdgcn_readfirstlane(threadIdx.x >> 6);
    STAMP_DECL; STAMP(0); RSTAMP(30);
    const int b = blockIdx.x, j = b >> 3;
    lds_ptr lds = (lds_ptr)smem; const unsigned lds0 = (unsigned)(uintptr_t)smem;
    if (w < 8) attention(P, lds, lds0, b, w, lane STAMP_PASS);
    else if (j < 28) { const int rbg = (b & 7) * 4 + j / 7, cbg = j % 7; gating<true>(P, lds, lds0, b, w - 8, lane, rbg * 64, cbg * 128 + (w - 8) * 32 STAMP_PASS); }
    else gating<false>(P, lds, lds0, b, w - 8, lane, 0, 0 STAMP_PASS);
    STAMP(17); RSTAMP(31); STAMP_FLUSH_K(1);
}
}
namespace k3 {
constexpr int BM = 96, A_BYTES = BM * 128, STAGE = 20480, NST = 7, NKS = 24, LDS_BYTES = NST * STAGE;
struct Params { const u16* x; const u16* wo; const u16* wout; const char* ws; u16* out; };
struct Ld { rsrc_t ra, rb0, rb1; unsigned va[2], vb[2], sa, sb, da, db; };
template <int S> DI void ld_issue(const Ld& L) {
    constexpr int so = (S % NST) * STAGE;
#pragma unroll
    for (int j = 0; j < 3; ++j) bdma(L.ra, L.va[j & 1], L.sa + S * 128 + j * 8 * 3072, L.da + so + j * 1024);
#pragma unroll
    for (int j = 0; j < 2; ++j) bdma(S < 10 ? L.rb0 : L.rb1, L.vb[j], L.sb + ((S < 10 ? S : S - 10) * 64 + j * 8) * 1280, L.db + so + j * 1024);
}
constexpr int issued_at(int S) { return (S < 3 ? 2 + 2 * S : S + 5) < NKS ? (S < 3 ? 2 + 2 * S : S + 5) : NKS - 1; }
template <int A, int B> DI void ld_range(const Ld& L) { if constexpr (A <= B) { ld_issue<A>(L); ld_range<A + 1, B>(L); } }
template <int S> DI void ld_step(const Ld& L) {
    if constexpr (S > 0) ld_range<issued_at(S - 1) + 1, issued_at(S)>(L);
    constexpr int young = 5 * (issued_at(S) - (S + 1));
    if constexpr (young == 20) WAIT_BAR(20); else if constexpr (young == 15) WAIT_BAR(15); else if constexpr (young == 10) WAIT_BAR(10);
    else if constexpr (young == 5) WAIT_BAR(5); else WAIT_BAR(0);
    if constexpr (S + 2 < NKS) ld_step<S + 1>(L);
}
struct Frag { u32x4 x[3], w[2]; };
struct Ctx { unsigned xo[3], wo0[3], wo1[3]; };
template <int S> DI void rd(Frag& f, lds_ptr lds, const Ctx& c) {
    constexpr int slot = S % NST, grp = slot / 3, so = (slot % 3) * STAGE;
#pragma unroll
    for (int n = 0; n < 2; ++n) {
        lds_ptr wp = lds + (n ? c.wo1[grp] : c.wo0[grp]) + so;
        const u32x2 lo = ldstr(wp), hi = ldstr(wp + 512);
        f.w[n] = u32x4{lo[0], lo[1], hi[0], hi[1]};
    }
#pragma unroll
    for (int m = 0; m < 3; ++m) f.x[m] = ldsr128(lds + c.xo[grp] + so + m * 2048);
}
template <int S> DI void stage(lds_ptr lds, const Ctx& c, Frag& fc, Frag& fn, f32x4 (&acc)[3][2] STAMP_ARG) {
    if constexpr (S + 1 < NKS) {
        BAR_LDS();
        STAMP(2 + S);
        rd<S + 1>(fn, lds, c);
        SBAR();
    }
#pragma unroll
    for (int m = 0; m < 3; ++m)
#pragma unroll
        for (int n = 0; n < 2; ++n) acc[m][n] = mfma16(fc.w[n], fc.x[m], acc[m][n]);
    SBAR();
    if constexpr (S + 1 < NKS) stage<S + 1>(lds, c, fn, fc, acc STAMP_PASS);
}

__global__ __launch_bounds__(768, 3) void k3_out(Params P) {
    extern __shared__ __attribute__((aligned(16))) char smem[];
    const int tid = threadIdx.x, lane = tid & 63, w = __builtin_amdgcn_readfirstlane(tid >> 6);
    STAMP_DECL; STAMP(0); RSTAMP(30);
    const int b = blockIdx.x, j0 = b >> 3, n0 = (b & 7) * 28 + j0;
    if (j0 >= 28 || n0 >= 220) return;
    const int half = n0 >= 110, r_ = n0 - half * 110, rb = r_ / 5, cb = half * 5 + r_ - rb * 5;
    const int row0 = rb * BM;
    lds_ptr lds = (lds_ptr)smem;
    const unsigned lds0 = (unsigned)(uintptr_t)smem;
    if (w >= 8) {
        const int L = w - 8, r8 = lane >> 3, c8 = lane & 7, k4 = lane >> 4;
        Ld ld;
        ld.ra = mk_rsrc(P.ws + OFF_OU, S_ * 3072); ld.rb0 = mk_rsrc(P.wo, D_ * 1280); ld.rb1 = mk_rsrc(P.wout, FH_ * 1280);
#pragma unroll
        for (int i = 0; i < 2; ++i) {
            ld.va[i] = r8 * 3072 + ((c8 ^ ((4 * ((L + i) & 1) + k4) & 7)) << 4);
            ld.vb[i] = r8 * 1280 + ((c8 ^ (((k4 & 1) | (i << 1)) << 1)) << 4);
        }
        ld.sa = (row0 + 24 * L) * 3072; ld.sb = cb * 128 + 16 * L * 1280;
        ld.da = lds0 + 3 * L * 1024; ld.db = lds0 + A_BYTES + 2 * L * 1024;
        STAMP(1);
        ld_issue<0>(ld); ld_issue<1>(ld); ld_issue<2>(ld);
        STAMP(2);
        WAIT_BAR(10);
        STAMP(3);
        ld_step<0>(ld);
        return;
    }
    const int kh = w >> 2, wr = w & 1, wc = (w >> 1) & 1;
    const int r15 = lane & 15, g = lane >> 4, q = r15 >> 2, p = lane & 3;
    Ctx c;
    const unsigned xoff = (48 * wr + r15) * 128 + (((4 * kh + g) ^ ((r15 >> 1) & 7)) << 4);
    const int fk = (((q >> 1) & 1) | ((g & 1) << 1)) << 1;
    const unsigned woff = A_BYTES + (32 * kh + 8 * g + q) * 128 + (((4 * wc + (p >> 1)) ^ fk) << 4) + 8 * (p & 1);
#pragma unroll
    for (int i = 0; i < 3; ++i) { c.xo[i] = xoff + i * 3 * STAGE; c.wo0[i] = woff + i * 3 * STAGE; c.wo1[i] = (woff ^ 32) + i * 3 * STAGE; }
    f32x4 acc[3][2];
#pragma unroll
    for (int m = 0; m < 3; ++m) { acc[m][0] = f32x4{0, 0, 0, 0}; acc[m][1] = f32x4{0, 0, 0, 0}; }
    Frag f0, f1;
    BAR_LDS();
    rd<0>(f0, lds, c);
    const int co = cb * 64 + wc * 32;
    u32x2 xr[2][2];
#pragma unroll
    for (int u = 0; u < 2; ++u) {
        const int m = kh ? 2 : u, t = min(row0 + 48 * wr + 16 * m + r15, S_ - 1);
#pragma unroll
        for (int n = 0; n < 2; ++n) xr[u][n] = *(const u32x2*)(P.x + (unsigned)(t * D_ + co + 16 * n + 4 * g));
    }

    stage<0>(lds, c, f0, f1, acc STAMP_PASS);
    STAMP(28);
    BAR_LDS();
    STAMP(25);
    LDSP f32x4* xch = (LDSP f32x4*)lds + (w & 3) * 6 * 64;
    if (kh == 1) {
#pragma unroll
        for (int m = 0; m < 2; ++m) { xch[(m * 2) * 64 + lane] = acc[m][0]; xch[(m * 2 + 1) * 64 + lane] = acc[m][1]; }
    } else { xch[4 * 64 + lane] = acc[2][0]; xch[5 * 64 + lane] = acc[2][1]; }
    BAR_LDS();
    STAMP(26);
#pragma unroll
    for (int u = 0; u < 2; ++u) {
        if (kh == 1 && u == 1) break;
        const int m = kh ? 2 : u;
        const int tb = row0 + 48 * wr + 16 * m;
        if (tb >= S_) break;
        const int t = tb + r15;
        unsigned pk[2][2];
#pragma unroll
        for (int n = 0; n < 2; ++n) {
            const f32x4 mine = kh ? acc[2][n] : acc[u][n];
            const f32x4 v = mine + xch[(m * 2 + n) * 64 + lane];
            pk[n][0] = pk2(v[0] + bflo(xr[u][n][0]), v[1] + bfhi(xr[u][n][0])); pk[n][1] = pk2(v[2] + bflo(xr[u][n][1]), v[3] + bfhi(xr[u][n][1]));
        }
        auto x0 = __builtin_amdgcn_permlane16_swap(pk[0][0], pk[1][0], false, false);
        auto x1 = __builtin_amdgcn_permlane16_swap(pk[0][1], pk[1][1], false, false);
        *(u32x4*)(P.out + (unsigned)(t * D_ + co + 16 * (g & 1) + 8 * (g >> 1))) = u32x4{x0[0], x1[0], x0[1], x1[1]};
    }
    STAMP(29); RSTAMP(31); STAMP_FLUSH_K(2);
}
}
extern "C" void kernel_launch(void* const* d_in, const int* in_sizes, int n_in, void* d_out, int out_size, void* d_ws, size_t ws_size,
                              hipStream_t stream) {
    (void)in_sizes; (void)n_in; (void)out_size; (void)ws_size;
    const u16* x = (const u16*)d_in[0];
    k1::Params p1{x, (const u16*)d_in[1], (const u16*)d_in[2], (const u16*)d_in[3], (const u16*)d_in[6], (char*)d_ws};
    k2::Params p2{(const u16*)d_in[7], (const u16*)d_in[8], (const u16*)d_in[4], (const u16*)d_in[9], (char*)d_ws};
    k3::Params p3{x, (const u16*)d_in[4], (const u16*)d_in[9], (const char*)d_ws, (u16*)d_out};
    (void)hipFuncSetAttribute((const void*)k1::k1_proj, hipFuncAttributeMaxDynamicSharedMemorySize, k1::LDS_BYTES);
    (void)hipFuncSetAttribute((const void*)k2::k2_mix, hipFuncAttributeMaxDynamicSharedMemorySize, k2::LDS_BYTES);
    (void)hipFuncSetAttribute((const void*)k3::k3_out, hipFuncAttributeMaxDynamicSharedMemorySize, k3::LDS_BYTES);
    k1::k1_proj<<<256, 768, k1::LDS_BYTES, stream>>>(p1);
    k2::k2_mix<<<256, 768, k2::LDS_BYTES, stream>>>(p2);
    k3::k3_out<<<256, 768, k3::LDS_BYTES, stream>>>(p3);
}
```

```cpp
#include <hip/hip_runtime.h>
#include <stdint.h>
#pragma clang diagnostic ignored "-Winline-asm"

typedef unsigned short u16;
using bf16x8 = __attribute__((ext_vector_type(8))) __bf16;
using bf16x2 = __attribute__((ext_vector_type(2))) __bf16;
using s16x4 = __attribute__((ext_vector_type(4))) short;
using f32x2 = __attribute__((ext_vector_type(2))) float;
using f32x4 = __attribute__((ext_vector_type(4))) float;
using f32x16 = __attribute__((ext_vector_type(16))) float;
using u32x2 = __attribute__((ext_vector_type(2))) unsigned;
using u32x4 = __attribute__((ext_vector_type(4))) unsigned;
#define DI __device__ __forceinline__
#define LDSP __attribute__((address_space(3)))
typedef LDSP char* lds_ptr;

constexpr int S_ = 2048, D_ = 640, FH_ = 864, F_ = 1728;
constexpr size_t OFF_Q = 0;
constexpr size_t OFF_KF = OFF_Q + (size_t)S_ * D_ * 2;
constexpr size_t OFF_VF = OFF_KF + 131072;
constexpr size_t OFF_Z1 = OFF_VF + 131072;
constexpr size_t OFF_Z2R = OFF_Z1 + (size_t)S_ * FH_ * 2;
constexpr size_t OFF_Z2N = OFF_Z2R + (size_t)S_ * FH_ * 2;
constexpr size_t OFF_OU = OFF_Z2N + (size_t)S_ * FH_ * 2 + 4096;
constexpr size_t OFF_FLAG = OFF_OU + (size_t)S_ * 1536 * 2;
constexpr int V_SLOTS = 22;
constexpr int KT_BYTES = 1536, VT_BYTES = 4 * V_SLOTS * 16;

DI float bf2f(u16 b) { return __uint_as_float(((unsigned)b) << 16); }
DI unsigned pk2(float lo, float hi) { bf16x2 v = {(__bf16)lo, (__bf16)hi}; return __builtin_bit_cast(unsigned, v); }
DI float bflo(unsigned v) { return __uint_as_float(v << 16); }
DI float bfhi(unsigned v) { return __uint_as_float(v & 0xffff0000u); }
DI void glds16(const void* g, unsigned lds_base) {
    unsigned sv;
    asm volatile("s_mov_b32 %0, m0\n\ts_mov_b32 m0, %2\n\ts_nop 0\n\tglobal_load_lds_dwordx4 %1, off\n\ts_mov_b32 m0, %0"
                 : "=&s"(sv) : "v"(g), "s"(lds_base) : "memory");
}
DI void glds(const void* g, unsigned lds_dst) {
    asm volatile("s_mov_b32 m0, %1\n\ts_nop 0\n\tglobal_load_lds_dwordx4 %0, off" :: "v"(g), "s"(lds_dst) : "memory", "m0");
}
DI void glds_lo32(const void* g, unsigned lds_dst) {
    asm volatile("s_mov_b32 m0, %1\n\ts_mov_b32 exec_hi, 0\n\tglobal_load_lds_dwordx4 %0, off\n\ts_mov_b32 exec_hi, -1" :: "v"(g), "s"(lds_dst) : "memory", "m0");
}
typedef __amdgpu_buffer_rsrc_t rsrc_t;
DI rsrc_t mk_rsrc(const void* p, unsigned bytes) { return __builtin_amdgcn_make_buffer_rsrc((void*)p, 0, bytes, 0x00020000); }
DI void bdma(rsrc_t r, unsigned voff, unsigned soff, unsigned lds_dst) {
    asm volatile("s_mov_b32 m0, %3\n\ts_nop 0\n\tbuffer_load_dwordx4 %0, %1, %2 offen lds" :: "v"(voff), "s"(r), "s"(soff), "s"(lds_dst) : "memory", "m0");
}
#define WAIT_VM(n) asm volatile("s_waitcnt vmcnt(" #n ")" ::: "memory")
#define WAIT_BAR(n) asm volatile("s_waitcnt vmcnt(" #n ") lgkmcnt(0)\n\ts_barrier" ::: "memory")
#define BAR_LDS() asm volatile("s_waitcnt lgkmcnt(0)\n\ts_barrier" ::: "memory")
#define SBAR() __builtin_amdgcn_sched_barrier(0)
DI u32x4 ldsr128(lds_ptr p) { return *(const LDSP u32x4*)p; }
typedef short v4i16_t __attribute__((ext_vector_type(4)));
DI u32x2 ldstr(lds_ptr p) { return __builtin_bit_cast(u32x2, __builtin_amdgcn_ds_read_tr16_b64_v4i16((LDSP v4i16_t*)p)); }
#define ST_AUX 16
DI void st128(rsrc_t r, unsigned off, u32x4 v) { __builtin_amdgcn_raw_buffer_store_b128(v, r, off, 0, ST_AUX); }
DI void st64(rsrc_t r, unsigned off, u32x2 v) { __builtin_amdgcn_raw_buffer_store_b64(v, r, off, 0, ST_AUX); }
DI void touch_lines8(rsrc_t r, unsigned voff, unsigned soff, unsigned step) {
    unsigned t0, t1, t2, t3, t4, t5, t6, t7;
    asm volatile("buffer_load_dword %0, %8, %9, %10 offen\n\tbuffer_load_dword %1, %8, %9, %11 offen\n\tbuffer_load_dword %2, %8, %9, %12 offen\n\t"
                 "buffer_load_dword %3, %8, %9, %13 offen\n\tbuffer_load_dword %4, %8, %9, %14 offen\n\tbuffer_load_dword %5, %8, %9, %15 offen\n\t"
                 "buffer_load_dword %6, %8, %9, %16 offen\n\tbuffer_load_dword %7, %8, %9, %17 offen\n\ts_waitcnt vmcnt(0)"
                 : "=&v"(t0), "=&v"(t1), "=&v"(t2), "=&v"(t3), "=&v"(t4), "=&v"(t5), "=&v"(t6), "=&v"(t7)
                 : "v"(voff), "s"(r), "s"(soff), "s"(soff + step), "s"(soff + 2 * step), "s"(soff + 3 * step), "s"(soff + 4 * step), "s"(soff + 5 * step),
                   "s"(soff + 6 * step), "s"(soff + 7 * step) : "memory");
}
DI void touch_line(unsigned& t, rsrc_t r, unsigned voff, unsigned soff) {
    asm volatile("buffer_load_dword %0, %1, %2, %3 offen" : "=v"(t) : "v"(voff), "s"(r), "s"(soff) : "memory");
}
#define TOUCH_DONE(t) asm volatile("" :: "v"(t))
DI f32x4 mfma16(u32x4 a, u32x4 b, f32x4 c) { return __builtin_amdgcn_mfma_f32_16x16x32_bf16(__builtin_bit_cast(bf16x8, a), __builtin_bit_cast(bf16x8, b), c, 0, 0, 0); }
DI void mfma16_acc(f32x4& acc, const u32x4& a, const u32x4& b) { asm volatile("v_mfma_f32_16x16x32_bf16 %0, %1, %2, %0" : "+v"(acc) : "v"(a), "v"(b)); }
DI f32x16 mfma32(u32x4 a, u32x4 b, f32x16 c) { return __builtin_amdgcn_mfma_f32_32x32x16_bf16(__builtin_bit_cast(bf16x8, a), __builtin_bit_cast(bf16x8, b), c, 0, 0, 0); }
DI f32x16 mfma32k8(u32x2 a, u32x2 b, f32x16 c) { return __builtin_amdgcn_mfma_f32_32x32x8bf16_1k(__builtin_bit_cast(s16x4, a), __builtin_bit_cast(s16x4, b), c, 0, 0, 0); }
DI float dot2(unsigned a, unsigned b, float c) { return __builtin_amdgcn_fdot2_f32_bf16(__builtin_bit_cast(bf16x2, a), __builtin_bit_cast(bf16x2, b), c, false); }
DI float gelu_tanh(float x) {
    const float c0 = -2.302208198f, c1 = -0.1029432397f;
    float e = __builtin_amdgcn_exp2f(x * __builtin_fmaf(x * x, c1, c0));
    return x * __builtin_amdgcn_rcpf(1.f + e);
}
#define STAMP_DECL
#define STAMP(i)
#define RSTAMP(i)
#define STAMP_FLUSH_K(k)
#define STAMP_ARG
#define STAMP_PASS
namespace k1 {
#define K1_EPRIO 1
#define USE1(x)
constexpr int BM = 160, A_BYTES = BM * 128, B_BYTES = 64 * 256, NKS = 10;
constexpr int L_A = 0, L_B = 4 * A_BYTES, L_ST = L_B + 4 * B_BYTES, LDS_BYTES = L_ST + 4096;
enum { T_Q = 0, T_K = 1, T_V = 2, T_Z1 = 3, T_Z2 = 4 };
struct Params { const u16* x; const u16* wq; const u16* wk; const u16* wv; const u16* win; const u16* wsp; char* ws; };

struct Ld { rsrc_t ra, rb, rp; unsigned va[2], vb[2], sa, sb, sbinc, da, db, vp, sp, tp; const char* pk[4]; unsigned pinc[4]; int rot; };
#define K1_PFS 99
template <bool KV, int S> DI void ld_issue(Ld& L) {
    int gs = S + L.rot; if (gs >= NKS) gs -= NKS;
#pragma unroll
    for (int j = 0; j < 5; ++j) bdma(L.ra, L.va[j & 1], L.sa + gs * 128 + j * 8 * 1280, L.da + (S & 3) * A_BYTES + j * 1024);
#pragma unroll
    for (int j = 0; j < 4; ++j) {
        if constexpr (KV) glds(L.pk[j] + (unsigned)gs * L.pinc[j], L.db + (S & 3) * B_BYTES + j * 1024);
        else bdma(L.rb, L.vb[(j >> 1) & 1], L.sb + gs * L.sbinc + j * (L.sbinc >> 4), L.db + (S & 3) * B_BYTES + j * 1024);
    }
}
template <bool KV, int S> DI void ld_step(Ld& L) {
    if constexpr (S == 1) { ld_issue<KV, 2>(L); ld_issue<KV, 3>(L); }
    else if constexpr (S >= 2 && S + 2 < NKS) ld_issue<KV, S + 2>(L);
    if constexpr (S == K1_PFS) touch_line(L.tp, L.rp, L.vp, L.sp);
    constexpr int issued = S == 0 ? 1 : (S + 2 < NKS ? S + 2 : NKS - 1);
    asm volatile("s_waitcnt vmcnt(%0) lgkmcnt(0)\n\ts_barrier" :: "i"(9 * (issued - (S + 1)) + (S == K1_PFS ? 1 : 0)) : "memory");
    if constexpr (S == K1_PFS + 1) TOUCH_DONE(L.tp);
    if constexpr (S + 2 < NKS) ld_step<KV, S + 1>(L);
}
template <bool KV> DI void loader(Ld& L STAMP_ARG) {
    ld_issue<KV, 0>(L); ld_issue<KV, 1>(L);
    STAMP(2);
    WAIT_BAR(9);
    STAMP(3);
    ld_step<KV, 0>(L);
}

struct Frag { u32x4 x[5], w[2]; };
template <int S, int KS> DI void rd(Frag& f, lds_ptr lds, const unsigned (&xo)[5], unsigned wo0, unsigned wo1) {
    constexpr int sa = (S & 3) * A_BYTES, sb = (S & 3) * B_BYTES + KS * 8192;
#pragma unroll
    for (int n = 0; n < 2; ++n) {
        lds_ptr wp = lds + (n ? wo1 : wo0) + sb;
        const u32x2 lo = ldstr(wp), hi = ldstr(wp + 1024);
        f.w[n] = u32x4{lo[0], lo[1], hi[0], hi[1]};
    }
#pragma unroll
    for (int m = 0; m < 5; ++m) f.x[m] = ldsr128(lds + (xo[m] ^ (KS * 64)) + sa);
}
struct Stat { f32x4 s0, q0, s1, q1, cs; };
template <int S, int KS, int I> DI void rd1(Frag& f, lds_ptr lds, const unsigned (&xo)[5], unsigned wo0, unsigned wo1) {
    constexpr int sa = (S & 3) * A_BYTES, sb = (S & 3) * B_BYTES + KS * 8192;
    if constexpr (I < 4) {
        const u32x2 t = ldstr(lds + ((I >> 1) ? wo1 : wo0) + sb + (I & 1) * 1024);
        f.w[I >> 1][2 * (I & 1)] = t[0]; f.w[I >> 1][2 * (I & 1) + 1] = t[1];
    } else f.x[I - 4] = ldsr128(lds + (xo[I - 4] ^ (KS * 64)) + sa);
}
template <bool FT, int KSG, bool RD, int S, int KS>
DI void mm(const Frag& fc, Frag& fn, lds_ptr lds, const unsigned (&xo)[5], unsigned wo0, unsigned wo1, f32x4 (&acc)[5][2], int wc, Stat& st, const u32x4& ones) {
#define K1_MM(i) do { if (FT) mfma16_acc(acc[(i) >> 1][(i) & 1], fc.w[(i) & 1], fc.x[(i) >> 1]); else mfma16_acc(acc[(i) >> 1][(i) & 1], fc.x[(i) >> 1], fc.w[(i) & 1]); } while (0)
#define K1_RD(i) do { if constexpr (RD) rd1<S, KS, i>(fn, lds, xo, wo0, wo1); SBAR(); } while (0)
    K1_MM(0); K1_RD(0); K1_MM(1); K1_RD(1); K1_MM(2); K1_RD(2); K1_MM(3); K1_RD(3); K1_MM(4); K1_RD(4);
    K1_MM(5); K1_RD(5); K1_MM(6); K1_RD(6); K1_MM(7); K1_RD(7); K1_MM(8); K1_RD(8); K1_MM(9);
#undef K1_MM
#undef K1_RD
    mfma16_acc(st.s0, fc.x[0], ones); mfma16_acc(st.q0, fc.x[0], fc.x[0]); mfma16_acc(st.cs, ones, fc.w[0]);
    if ((KSG & 3) == wc) { mfma16_acc(st.s1, fc.x[4], ones); mfma16_acc(st.q1, fc.x[4], fc.x[4]); }
    SBAR();
}
template <bool FT, int S>
DI void stage(lds_ptr lds, Frag& f0, Frag& f1, const unsigned (&xo)[5], unsigned wo0, unsigned wo1, int wc,
              f32x4 (&acc)[5][2], Stat& st, const u32x4& ones STAMP_ARG) {
    if constexpr (S + 1 < NKS) BAR_LDS();
    STAMP(3 + S);
    mm<FT, 2 * S, true, S, 1>(f0, f1, lds, xo, wo0, wo1, acc, wc, st, ones);
    mm<FT, 2 * S + 1, (S + 1 < NKS), S + 1, 0>(f1, f0, lds, xo, wo0, wo1, acc, wc, st, ones);
    if constexpr (S + 1 < NKS) stage<FT, S + 1>(lds, f0, f1, xo, wo0, wo1, wc, acc, st, ones STAMP_PASS);
}
template <bool FT>
DI void mainloop(lds_ptr lds, int lane, int wr, int wc, f32x4 (&acc)[5][2], Stat& st STAMP_ARG) {
    const int r15 = lane & 15, g = lane >> 4;
    const unsigned xb = L_A + (80 * wr + r15) * 128 + ((g ^ ((r15 >> 1) & 7)) << 4);
    unsigned xo[5];
#pragma unroll
    for (int m = 0; m < 4; ++m) xo[m] = xb + (m ^ wc) * 2048;
    xo[4] = xb + 8192;
    const int q = r15 >> 2, p = lane & 3;
    const int fk = (q << 1) | ((g & 1) << 3);
    const unsigned wo0 = (L_B + (8 * g + q) * 256 + (((4 * wc + (p >> 1)) ^ fk) << 4) + 8 * (p & 1)) ^ (wr * 32), wo1 = wo0 ^ 32;
    Frag f0, f1;
    u32x4 ones = u32x4{0x3F803F80u, 0x3F803F80u, 0x3F803F80u, 0x3F803F80u};
    asm volatile("" : "+v"(ones), "+v"(st.s0), "+v"(st.q0), "+v"(st.s1), "+v"(st.q1), "+v"(st.cs));
#pragma unroll
    for (int m = 0; m < 5; ++m) asm volatile("" : "+v"(acc[m][0]), "+v"(acc[m][1]));
    BAR_LDS();
    rd<0, 0>(f0, lds, xo, wo0, wo1);
    stage<FT, 0>(lds, f0, f1, xo, wo0, wo1, wc, acc, st, ones STAMP_PASS);
    asm volatile("s_nop 7\n\ts_nop 7" ::: "memory");
}
template <int N> DI void gelu_n(f32x2* v) {
    f32x2 t[N];
#pragma unroll
    for (int i = 0; i < N; ++i) t[i] = v[i] * __builtin_elementwise_fma(v[i] * v[i], f32x2{-0.1029432397f, -0.1029432397f}, f32x2{-2.302208198f, -2.302208198f});
    SBAR();
#pragma unroll
    for (int i = 0; i < N; ++i) t[i] = f32x2{__builtin_amdgcn_exp2f(t[i][0]), __builtin_amdgcn_exp2f(t[i][1])};
    SBAR();
#pragma unroll
    for (int i = 0; i < N; ++i) t[i] += f32x2{1.f, 1.f};
#pragma unroll
    for (int i = 0; i < N; ++i) t[i] = f32x2{__builtin_amdgcn_rcpf(t[i][0]), __builtin_amdgcn_rcpf(t[i][1])};
    SBAR();
#pragma unroll
    for (int i = 0; i < N; ++i) v[i] *= t[i];
}
template <int TYPE>
DI void epilogue(char* ws, const f32x4 (&acc)[5][2], const LDSP float* csum, const LDSP float* ra, const LDSP float* rbv, int tw, int col0, int lane, int wr, int wc STAMP_ARG) {
    const int r15 = lane & 15, g = lane >> 4;
    constexpr bool FT = (TYPE == T_Q || TYPE == T_K || TYPE == T_Z1);
    float cf[2][4];
#pragma unroll
    for (int n = 0; n < 2; ++n) {
        if constexpr (FT) { const f32x4 c4 = *(const LDSP f32x4*)(csum + 32 * wc + 16 * (n ^ wr) + 4 * g); for (int j = 0; j < 4; ++j) cf[n][j] = c4[j]; }
        else cf[n][0] = csum[32 * wc + 16 * (n ^ wr) + r15];
    }
    f32x4 av[5], bv[5];
#pragma unroll
    for (int mp = 0; mp < 5; ++mp) {
        const int m = mp < 4 ? (mp ^ wc) : 4;
        if constexpr (FT) { av[mp][0] = ra[80 * wr + 16 * m + r15]; bv[mp][0] = rbv[80 * wr + 16 * m + r15]; }
        else { av[mp] = *(const LDSP f32x4*)(ra + 80 * wr + 16 * m + 4 * g); bv[mp] = *(const LDSP f32x4*)(rbv + 80 * wr + 16 * m + 4 * g); }
    }
    USE1(av[4][0]); USE1(bv[4][0]); STAMP(16);
    const rsrc_t rws = mk_rsrc(ws, (unsigned)OFF_Z2N);
#pragma unroll
    for (int mp = 0; mp < 5; ++mp) {
        const int m = mp < 4 ? (mp ^ wc) : 4;
        const int tb = tw + 16 * m;
        if (((wc >> 1) ^ mp) & 1) __builtin_amdgcn_s_setprio(1); else __builtin_amdgcn_s_setprio(0);
        f32x2 vv[4];
#pragma unroll
        for (int n = 0; n < 2; ++n)
#pragma unroll
            for (int j = 0; j < 2; ++j) {
                constexpr float sc = (TYPE == T_Q) ? 0.32259534218f : 1.f;
                const f32x2 ac = f32x2{acc[mp][n][2 * j], acc[mp][n][2 * j + 1]};
                if constexpr (FT) { const float a = av[mp][0] * sc, bb = bv[mp][0] * sc;
                                    vv[2 * n + j] = __builtin_elementwise_fma(f32x2{a, a}, ac, f32x2{bb, bb} * f32x2{cf[n][2 * j], cf[n][2 * j + 1]}); }
                else vv[2 * n + j] = __builtin_elementwise_fma(f32x2{av[mp][2 * j], av[mp][2 * j + 1]}, ac, f32x2{bv[mp][2 * j], bv[mp][2 * j + 1]} * f32x2{cf[n][0], cf[n][0]});
            }
        if constexpr (TYPE == T_Z1 || TYPE == T_Z2) { SBAR(); gelu_n<4>(vv); }
        const unsigned pa0 = pk2(vv[0][0], vv[0][1]), pa1 = pk2(vv[1][0], vv[1][1]), pb0 = pk2(vv[2][0], vv[2][1]), pb1 = pk2(vv[3][0], vv[3][1]);
        unsigned t00 = wr ? pb0 : pa0, t01 = wr ? pb1 : pa1, t10 = wr ? pa0 : pb0, t11 = wr ? pa1 : pb1;
        if constexpr (TYPE != T_K && TYPE != T_V) {
            auto x0 = __builtin_amdgcn_permlane16_swap(t00, t10, false, false);
            auto x1 = __builtin_amdgcn_permlane16_swap(t01, t11, false, false);
            t00 = x0[0]; t01 = x1[0]; t10 = x0[1]; t11 = x1[1];
        }
        asm volatile("" : "+v"(t00), "+v"(t01), "+v"(t10), "+v"(t11));
        if (tb < S_) {
            if constexpr (TYPE == T_K) {
                const int t = tb + r15;
                const unsigned kt = (unsigned)OFF_KF + (t >> 5) * KT_BYTES + (t & 31) * 16;
                st64(rws, kt + (g >> 1) * 512 + (g & 1) * 8, u32x2{t00, t01});
                if (g == 1) st64(rws, kt + 1024, u32x2{t10, t11});
                if (g == 2) st64(rws, kt + 1024 + 8, u32x2{0x00003F80u, 0u});
            } else if constexpr (TYPE == T_V) {
                const unsigned vt = (unsigned)OFF_VF + (tb >> 5) * VT_BYTES + ((((tb >> 4) & 1) * 2 + (g & 1)) * V_SLOTS) * 16 + (g >> 1) * 8;
                st64(rws, vt + r15 * 16, u32x2{t00, t01});
                if (r15 >= 4 && r15 < 8) st64(rws, vt + (12 + r15) * 16, u32x2{t10, t11});
                if (r15 == 8) st64(rws, vt + 20 * 16, u32x2{0x3F803F80u, 0x3F803F80u});
                if (r15 == 9) st64(rws, vt + 21 * 16, u32x2{0u, 0u});
            } else {
                unsigned dst;
                if constexpr (TYPE == T_Z2) dst = (unsigned)OFF_Z2R + (unsigned)(((tb >> 3) + (g >> 1)) * FH_ + col0 + 16 * (g & 1) + r15) * 16;
                else dst = (unsigned)(TYPE == T_Q ? OFF_Q : OFF_Z1) + (unsigned)((tb + r15) * (TYPE == T_Q ? 1280 : 1728) + (col0 + 16 * (g & 1) + 8 * (g >> 1)) * 2);
                st128(rws, dst, u32x4{t00, t01, t10, t11});
            }
        }
    }
}

__global__ __launch_bounds__(768, 3) void k1_proj(Params P) {
    extern __shared__ __attribute__((aligned(16))) char smem[];
    const int tid = threadIdx.x, lane = tid & 63, w = __builtin_amdgcn_readfirstlane(tid >> 6);
    STAMP_DECL; STAMP(0); RSTAMP(30);
    const int b = blockIdx.x, j0 = b >> 3, n0 = (b & 7) * 31 + j0;
    if (j0 == 31 || n0 >= 247) {
        if (b == 255 && tid < 64) ((u32x4*)(P.ws + OFF_FLAG))[tid * 8] = u32x4{0u, 0u, 0u, 0u};
        return;
    }
    int rb, cb;
    if (n0 < 133) { cb = n0 / 7; rb = n0 - cb * 7; } else { const int m = n0 - 133; cb = m / 6; rb = 7 + m - cb * 6; }
    const int row0 = rb * BM;
    lds_ptr lds = (lds_ptr)smem;
    const unsigned lds0 = (unsigned)(uintptr_t)smem;
    if (w >= 8) {
        const int L = w - 8;
        Ld ld;
        ld.ra = mk_rsrc(P.x, S_ * 1280);
        const int r8 = lane >> 3, c8 = lane & 7, k4 = lane >> 4, c16 = lane & 15;
#pragma unroll
        for (int i = 0; i < 2; ++i) ld.va[i] = r8 * 1280 + ((c8 ^ ((4 * ((L + i) & 1) + k4) & 7)) << 4);
        ld.sa = (row0 + 40 * L) * 1280;
        ld.da = lds0 + L_A + 5 * L * 1024; ld.db = lds0 + L_B + 4 * L * 1024;
        const unsigned ldw = cb < 5 ? 1280u : 3456u;
        ld.rb = cb < 5 ? mk_rsrc(P.wq, D_ * 1280) : mk_rsrc(P.win, D_ * 3456);
#pragma unroll
        for (int i = 0; i < 2; ++i) ld.vb[i] = k4 * ldw + ((c16 ^ (k4 << 1) ^ (i << 3)) << 4);
        ld.sb = (cb < 5 ? cb : cb - 5) * 256 + 16 * L * ldw; ld.sbinc = 64 * ldw;
        ld.rot = 0;
        ld.rp = mk_rsrc(P.wsp, S_ * 4096); ld.vp = (lane >> 5) * 4096 + (lane & 31) * 128; ld.sp = (unsigned)(b * 8 + L * 2) * 4096; ld.tp = 0;
        STAMP(1);
        if (cb != 18) loader<false>(ld STAMP_PASS);
        else {
#pragma unroll
            for (int j = 0; j < 4; ++j) {
                const int k = 16 * L + 4 * j + k4, c = c16 ^ (k4 << 1) ^ (((j >> 1) & 1) << 3), cc = c & 3;
                if (c < 8) { ld.pk[j] = (const char*)P.win + k * 3456 + 13 * 256 + c * 16; ld.pinc[j] = 64 * 3456; }
                else { ld.pk[j] = (const char*)(c < 12 ? P.wk : P.wv) + k * 40 + (cc == 1 ? 16 : cc == 2 ? 24 : 0); ld.pinc[j] = 64 * 40; }
            }
            loader<true>(ld STAMP_PASS);
        }
        return;
    }
    unsigned tch = 0;
    {
        const int wg = n0;
        if (w == 0) touch_line(tch, mk_rsrc(P.x, S_ * 1280), (unsigned)(wg * 83 + lane) * 128, 0);
        else if (w == 1) touch_line(tch, mk_rsrc(P.x, S_ * 1280), (unsigned)(wg * 83 + min(64 + lane, 82)) * 128, 0);
        else if (w == 2) touch_line(tch, mk_rsrc(P.win, D_ * 3456), (unsigned)(wg * 70 + lane) * 128, 0);
        else if (w == 3) touch_line(tch, mk_rsrc(P.win, D_ * 3456), (unsigned)(wg * 70 + min(64 + lane, 69)) * 128, 0);
        else if (w == 4) touch_line(tch, mk_rsrc(P.wq, D_ * 1280), (unsigned)(wg * 26 + min(lane, 25)) * 128, 0);
    }
    const int wr = w & 1, wc = w >> 1;
    int type, col0;
    if (cb < 5) { type = T_Q; col0 = cb * 128 + wc * 32; }
    else { const int zc = (cb - 5) * 128 + wc * 32;
           if (cb == 18 && wc >= 2) { type = (wc == 2) ? T_K : T_V; col0 = 0; }
           else if (zc < FH_) { type = T_Z1; col0 = zc; } else { type = T_Z2; col0 = zc - FH_; } }
    const bool ft = (type == T_Q || type == T_K || type == T_Z1);
    f32x4 acc[5][2];
#pragma unroll
    for (int m = 0; m < 5; ++m) { acc[m][0] = f32x4{0, 0, 0, 0}; acc[m][1] = f32x4{0, 0, 0, 0}; }
    Stat st; st.s0 = st.q0 = st.s1 = st.q1 = st.cs = f32x4{0, 0, 0, 0};
    STAMP(2);
    if (ft) mainloop<true>(lds, lane, wr, wc, acc, st STAMP_PASS);
    else mainloop<false>(lds, lane, wr, wc, acc, st STAMP_PASS);
    WAIT_VM(0); TOUCH_DONE(tch);
    STAMP(13);
    const int r15 = lane & 15, g = lane >> 4;
    LDSP float* rsum = (LDSP float*)(lds + L_ST); LDSP float* rsq = rsum + 160; LDSP float* csum = rsum + 320; LDSP float* ra = rsum + 448; LDSP float* rbv = rsum + 608;
    LDSP float* p4s = rsum + 768; LDSP float* p4q = rsum + 896;
    if (r15 == 0) { *(LDSP f32x4*)(rsum + 80 * wr + 16 * wc + 4 * g) = st.s0; *(LDSP f32x4*)(p4s + wc * 32 + wr * 16 + 4 * g) = st.s1; }
    if ((r15 >> 2) == g) {
        const int j = r15 & 3;
        rsq[80 * wr + 16 * wc + r15] = j == 0 ? st.q0[0] : j == 1 ? st.q0[1] : j == 2 ? st.q0[2] : st.q0[3];
        p4q[wc * 32 + wr * 16 + r15] = j == 0 ? st.q1[0] : j == 1 ? st.q1[1] : j == 2 ? st.q1[2] : st.q1[3];
    }
    if (g == 0) csum[32 * wc + 16 * wr + r15] = st.cs[0];
    STAMP(19);
    BAR_LDS();
    STAMP(20);
    if (tid < BM) {
        const int tr = tid >= 80, tl = tid - 80 * tr, o = tr * 16 + (tl & 15);
        float sm = rsum[tid], sq = rsq[tid];
        const float s4 = p4s[o] + p4s[32 + o] + p4s[64 + o] + p4s[96 + o], q4 = p4q[o] + p4q[32 + o] + p4q[64 + o] + p4q[96 + o];
        if (tl >= 64) { sm = s4; sq = q4; }
        const float mu = sm * (1.f / D_), var = fmaxf(sq * (1.f / D_) - mu * mu, 0.f);
        const float rs = __builtin_amdgcn_rsqf(var + 1e-5f);
        ra[tid] = rs; rbv[tid] = -mu * rs;
    }
    BAR_LDS();
    STAMP(14);
    const int tw = row0 + 80 * wr;
    switch (type) {
        case T_Q: epilogue<T_Q>(P.ws, acc, csum, ra, rbv, tw, col0, lane, wr, wc STAMP_PASS); break;
        case T_K: epilogue<T_K>(P.ws, acc, csum, ra, rbv, tw, col0, lane, wr, wc STAMP_PASS); break;
        case T_V: epilogue<T_V>(P.ws, acc, csum, ra, rbv, tw, col0, lane, wr, wc STAMP_PASS); break;
        case T_Z1: epilogue<T_Z1>(P.ws, acc, csum, ra, rbv, tw, col0, lane, wr, wc STAMP_PASS); break;
        default: epilogue<T_Z2>(P.ws, acc, csum, ra, rbv, tw, col0, lane, wr, wc STAMP_PASS); break;
    }
    STAMP(15); RSTAMP(31); STAMP_FLUSH_K(0);
}
}
namespace k2 {
constexpr int KV_HALF = 12288, KV_STAGE = 2 * KV_HALF, W_STAGE = 32768;
constexpr int L_KV = 0, L_W = 3 * KV_STAGE, L_RED = L_W + 2 * W_STAGE, L_DUMMY = L_RED + 1024, L_OUT = L_DUMMY + 6144, LDS_BYTES = L_OUT + 8 * 1280;
enum { C_KV0 = 64, C_KV12, C_GAT, C_NORM, C_PUB, C_FLAG, C_ATT = 72  , C_PAIR = 80, C_PROG = 88  , C_UST = 70 };
DI void lds_add(lds_ptr lds, int word) {
    asm volatile("s_mov_b64 exec, 1\n\tds_add_u32 %0, %1\n\ts_mov_b64 exec, -1" :: "v"((unsigned)(L_RED + 4 * word) + (unsigned)(uintptr_t)lds), "v"(1u) : "memory");
}
DI void lds_wait_ge(lds_ptr lds, int word, unsigned target) {
    const volatile LDSP unsigned* p = (const volatile LDSP unsigned*)(lds + L_RED + 4 * word);
    for (unsigned spin = 0; spin < (1u << 26); ++spin) {
        if ((unsigned)__builtin_amdgcn_readfirstlane(*p) >= target) break;
        __builtin_amdgcn_s_sleep(1);
    }
    asm volatile("" ::: "memory");
}
constexpr float M_SHIFT = 100.f;
constexpr int ZSTEP = 2 * FH_ * 16;
struct Params { const u16* wsp; const u16* bsp; const u16* wo; const u16* wout; const u16* x; char* ws; };
#define USE(x) asm volatile("" :: "v"(x))
#define EXP2(x) __builtin_amdgcn_exp2f(x)
DI float rowmax32(const f32x16& s) {
    float a = fmaxf(fmaxf(s[0], s[1]), s[2]), b = fmaxf(fmaxf(s[3], s[4]), s[5]);
#pragma unroll
    for (int r = 6; r < 14; r += 4) { a = fmaxf(fmaxf(a, s[r]), s[r + 1]); b = fmaxf(fmaxf(b, s[r + 2]), s[r + 3]); }
    a = fmaxf(fmaxf(a, s[14]), s[15]); a = fmaxf(a, b);
    auto rr = __builtin_amdgcn_permlane32_swap(__float_as_uint(a), __float_as_uint(a), false, false);
    return fmaxf(__uint_as_float(rr[0]), __uint_as_float(rr[1]));
}
DI void pack16(const f32x16& s, u32x4& p0, u32x4& p1) {
    p0 = u32x4{pk2(s[0], s[1]), pk2(s[2], s[3]), pk2(s[4], s[5]), pk2(s[6], s[7])};
    p1 = u32x4{pk2(s[8], s[9]), pk2(s[10], s[11]), pk2(s[12], s[13]), pk2(s[14], s[15])};
}
DI f32x16 attn_fallback(const char* ws, u32x4 q0, u32x2 q1, int lane) {
    const int r = lane & 31, h = lane >> 5;
    if (h) q1 = u32x2{0u, 0u};
    f32x16 o = {}; float m = -INFINITY;
#pragma unroll 1
    for (int T = 0; T < 64; ++T) {
        const char* kt = ws + OFF_KF + T * KT_BYTES; const char* vt = ws + OFF_VF + T * VT_BYTES;
        const u32x4 k0 = *(const u32x4*)(kt + h * 512 + r * 16); const u32x2 k1 = *(const u32x2*)(kt + 1024 + r * 16 + h * 8);
        const int rv = min(r, 21);
        const u32x4 v0 = *(const u32x4*)(vt + (h * V_SLOTS + rv) * 16), v1 = *(const u32x4*)(vt + ((2 + h) * V_SLOTS + rv) * 16);
        f32x16 s = mfma32(k0, q0, f32x16{}); s = mfma32k8(k1, q1, s);
        const float mn = fmaxf(m, rowmax32(s));
        const float al = (mn == -INFINITY) ? 1.f : EXP2(m - mn);
        const float ms = (mn == -INFINITY) ? 0.f : mn;
#pragma unroll
        for (int i = 0; i < 16; ++i) { s[i] = EXP2(s[i] - ms); o[i] *= al; }
        m = mn;
        u32x4 p0, p1; pack16(s, p0, p1);
        o = mfma32(v0, p0, o); o = mfma32(v1, p1, o);
    }
    return o;
}

struct St {
    u32x4 qa0, qb0; u32x2 qa1, qb1;
    f32x16 sa, sb, oa, ob;
    u32x4 pa0, pa1, pb0, pb1;
    u32x4 k0[2]; u32x2 k1[2]; u32x4 v0[2], v1[2];
};
#define K2_SCHED 4
#define E_(S, i) S[i] = EXP2(S[i]); USE(S[i])
#define C_(P, j, S, i) P[j] = pk2(S[i], S[i + 1]); USE(P[j])
#define K2_STEP(S, P0, P1, SP, PP1, M0, M1, M2, M3, R0, R1, R2)                                          \
    do {                                                                                                 \
        C_(PP1, 3, SP, 14); M0; R0; SBAR(); E_(S, 0); E_(S, 1); E_(S, 2); E_(S, 3); SBAR();              \
        M1; R1; C_(P0, 0, S, 0); E_(S, 4); E_(S, 5); C_(P0, 1, S, 2); E_(S, 6); E_(S, 7); SBAR();        \
        M2; R2; C_(P0, 2, S, 4); E_(S, 8); E_(S, 9); C_(P0, 3, S, 6); E_(S, 10); E_(S, 11); SBAR();      \
        M3; C_(P1, 0, S, 8); E_(S, 12); E_(S, 13); C_(P1, 1, S, 10); E_(S, 14); E_(S, 15); C_(P1, 2, S, 12); SBAR(); \
    } while (0)
#define K2_FLIP 1
#define K2_PLO 1
#define K2_PHI 2
#define K2_ALIGN 1
template <int STEP> DI void prio_step() {
    if constexpr (STEP == 0) __builtin_amdgcn_s_setprio(3);
    if constexpr (STEP == 3) __builtin_amdgcn_s_setprio(2);
    if constexpr (STEP == 6) __builtin_amdgcn_s_setprio(1);
}
struct Pr { volatile LDSP unsigned* mine; const volatile LDSP unsigned* other; unsigned it, seen; };
DI void prio_rel(Pr& p) {
}
template <int I>
DI void att_iter(St& s, Pr& pr, lds_ptr pk0, lds_ptr pk1, lds_ptr pv) {
    constexpr int c = I & 1, n = c ^ 1;
    prio_rel(pr);
    prio_step<2 * I>();
    K2_STEP(s.sa, s.pa0, s.pa1, s.sb, s.pb1,
            s.sb = mfma32(s.k0[c], s.qb0, f32x16{}),
            s.sb = mfma32k8(s.k1[c], s.qb1, s.sb),
            s.ob = mfma32(s.v0[n], s.pb0, s.ob),
            s.ob = mfma32(s.v1[n], s.pb1, s.ob),
            { s.k0[n] = ldsr128(pk0 + I * KT_BYTES); s.k1[n] = *(const LDSP u32x2*)(pk1 + I * KT_BYTES); },
            { s.v0[c] = ldsr128(pv + I * VT_BYTES); },
            { s.v1[c] = ldsr128(pv + I * VT_BYTES + 2 * V_SLOTS * 16); });
    prio_step<2 * I + 1>();
    K2_STEP(s.sb, s.pb0, s.pb1, s.sa, s.pa1,
            s.sa = mfma32(s.k0[n], s.qa0, f32x16{}),
            s.sa = mfma32k8(s.k1[n], s.qa1, s.sa),
            s.oa = mfma32(s.v0[c], s.pa0, s.oa),
            s.oa = mfma32(s.v1[c], s.pa1, s.oa),
            {}, {}, {});
}
DI void att_loop(St& s, lds_ptr lds, lds_ptr kvb, unsigned k0o, unsigned k1o, unsigned vo, int w STAMP_ARG) {
    int buf = 0;
    Pr pr; pr.mine = (volatile LDSP unsigned*)(lds + L_RED) + C_PROG + w; pr.other = (const volatile LDSP unsigned*)(lds + L_RED) + C_PROG + (w ^ 4); pr.it = 0; pr.seen = 0;
    const volatile LDSP unsigned* cw = (const volatile LDSP unsigned*)(lds + L_RED);
    unsigned seen = 0;
#pragma unroll 1
    for (int ss = 0; ss < 8; ++ss) {
        const int word = ss == 0 ? C_KV0 : ss == 1 ? C_KV12 : C_GAT;
        const unsigned target = ss == 1 ? 8u : ss < 3 ? 0u : 4u * (ss - 1);
        if ((unsigned)__builtin_amdgcn_readfirstlane(seen) < target) lds_wait_ge(lds, word, target);
        if (ss > 0) lds_wait_ge(lds, C_ATT + ss - 1, 8u);
        lds_ptr kv = kvb + buf * KV_STAGE;
        att_iter<0>(s, pr, kv + k0o, kv + k1o, kv + vo);
        if (ss == 0) { WAIT_VM(0); lds_add(lds, C_KV12); }
        att_iter<1>(s, pr, kv + k0o, kv + k1o, kv + vo);
        att_iter<2>(s, pr, kv + k0o, kv + k1o, kv + vo);
        seen = cw[ss == 0 ? C_KV12 : C_GAT];
        att_iter<3>(s, pr, kv + k0o, kv + k1o, kv + vo);
        asm volatile("s_waitcnt lgkmcnt(0)" ::: "memory");
        lds_add(lds, C_ATT + ss);
        buf = (buf == 2) ? 0 : buf + 1;
    }
}
DI void attention(const Params& P, lds_ptr lds, unsigned lds0, int b, int w, int lane STAMP_ARG) {
    const int p = w & 3, hf = w >> 2, r31 = lane & 31, h = lane >> 5;
    char* ws = P.ws;
    St s;
    __builtin_amdgcn_s_setprio(2);
    BAR_LDS();
    const rsrc_t rkv = mk_rsrc(ws, (unsigned)OFF_Z1);
    unsigned skv[3], kinc[3];
#pragma unroll
    for (int i = 0; i < 3; ++i) {
        const int pc = 3 * w + i, ph = pc >= 12, j = pc - ph * 12;
        skv[i] = j < 6 ? (unsigned)OFF_KF + (ph * 32 + 1) * KT_BYTES + j * 1024 : (unsigned)OFF_VF + (ph * 32) * VT_BYTES + (j - 6) * 1024;
        kinc[i] = j < 6 ? 4 * KT_BYTES : 4 * VT_BYTES;
    }
    const unsigned kvdst = lds0 + L_KV + 3 * w * 1024;
    STAMP(18);
#pragma unroll
    for (int i = 0; i < 3; ++i) bdma(rkv, lane * 16, skv[i], kvdst + i * 1024);
#pragma unroll
    for (int i = 0; i < 2; ++i) {
        const int pc = min(2 * w + i, 13);
        const unsigned src = pc < 10 ? (unsigned)OFF_Q + (8 * b) * 1280 + pc * 1024 : (unsigned)OFF_KF + (pc >= 12 ? 32 * KT_BYTES : 0) + (pc & 1) * 1024;
        bdma(rkv, lane * 16, src, lds0 + L_W + W_STAGE + pc * 1024);
    }
    STAMP(19);
    WAIT_VM(0);
    lds_add(lds, C_KV0);
#pragma unroll
    for (int st = 1; st < 3; ++st)
#pragma unroll
        for (int i = 0; i < 3; ++i) bdma(rkv, lane * 16, skv[i] + st * kinc[i], kvdst + st * KV_STAGE + i * 1024);
    STAMP(20);
    lds_wait_ge(lds, C_KV0, 8u);
    STAMP(21);
    const int tA = 8 * b + 2 * p;
    {
        lds_ptr qp = lds + L_W + W_STAGE + (2 * p) * 1280 + r31 * 40;
        auto rd16 = [&](lds_ptr a) { const u32x2 lo = *(const LDSP u32x2*)a, hi = *(const LDSP u32x2*)(a + 8); return u32x4{lo[0], lo[1], hi[0], hi[1]}; };
        s.qa0 = rd16(qp + h * 16); s.qb0 = rd16(qp + 1280 + h * 16);
        s.qa1 = *(const LDSP u32x2*)(qp + 32); s.qb1 = *(const LDSP u32x2*)(qp + 1280 + 32);
        lds_ptr k0p = lds + L_W + W_STAGE + 10240;
        const u32x4 k00 = ldsr128(k0p + h * 512 + r31 * 16); const u32x2 k01 = *(const LDSP u32x2*)(k0p + 1024 + r31 * 16 + h * 8);
        lds_ptr kfp = k0p + hf * 2048;
        s.k0[0] = ldsr128(kfp + h * 512 + r31 * 16); s.k1[0] = *(const LDSP u32x2*)(kfp + 1024 + r31 * 16 + h * 8);
        if (h) { s.qa1 = u32x2{0u, 0u}; s.qb1 = u32x2{0u, 0u}; }
        f32x16 a = mfma32(k00, s.qa0, f32x16{}), c = mfma32(k00, s.qb0, f32x16{});
        a = mfma32k8(k01, s.qa1, a); c = mfma32k8(k01, s.qb1, c);
        const float ma = rowmax32(a) + M_SHIFT, mb = rowmax32(c) + M_SHIFT;
        if (h) { s.qa1[0] = pk2(-ma, 0.f); s.qb1[0] = pk2(-mb, 0.f); }
    }
    s.sa = mfma32(s.k0[0], s.qa0, f32x16{}); s.sa = mfma32k8(s.k1[0], s.qa1, s.sa);
    s.oa = f32x16{}; s.ob = f32x16{}; s.sb = f32x16{};
    s.pb0 = u32x4{0, 0, 0, 0}; s.pb1 = u32x4{0, 0, 0, 0}; s.v0[1] = u32x4{0, 0, 0, 0}; s.v1[1] = u32x4{0, 0, 0, 0};
    lds_ptr kvb = lds + L_KV + hf * KV_HALF;
    const unsigned k0o = h * 512 + r31 * 16, k1o = 1024 + r31 * 16 + h * 8, vo = 6144 + (h * V_SLOTS + min(r31, 21)) * 16;
    STAMP(3);
    att_loop(s, lds, kvb, k0o, k1o, vo, w STAMP_PASS);
    STAMP(15);
    s.pb1[3] = pk2(s.sb[14], s.sb[15]);
    s.ob = mfma32(s.v0[1], s.pb0, s.ob); s.ob = mfma32(s.v1[1], s.pb1, s.ob);
    __builtin_amdgcn_s_setprio(3);
    lds_wait_ge(lds, C_ATT + 5, 8u);
    LDSP f32x4* xs = (LDSP f32x4*)(lds + L_KV + 2 * KV_STAGE) + w * 3 * 64;
    LDSP f32x4* xr = (LDSP f32x4*)(lds + L_KV + 2 * KV_STAGE) + (w ^ 4) * 3 * 64;
    if (hf == 0) {
#pragma unroll
        for (int i = 0; i < 3; ++i) xs[i * 64 + lane] = f32x4{s.ob[4 * i], s.ob[4 * i + 1], s.ob[4 * i + 2], s.ob[4 * i + 3]};
    } else {
#pragma unroll
        for (int i = 0; i < 3; ++i) xs[i * 64 + lane] = f32x4{s.oa[4 * i], s.oa[4 * i + 1], s.oa[4 * i + 2], s.oa[4 * i + 3]};
        s.oa = s.ob; s.qa0 = s.qb0; s.qa1 = s.qb1;
    }
    asm volatile("s_waitcnt lgkmcnt(0)" ::: "memory");
    lds_add(lds, C_PAIR + w);
    lds_wait_ge(lds, C_PAIR + (w ^ 4), 1u);
    STAMP(16);
    f32x16 o = s.oa;
#pragma unroll
    for (int i = 0; i < 3; ++i) { const f32x4 t = xr[i * 64 + lane]; o[4 * i] += t[0]; o[4 * i + 1] += t[1]; o[4 * i + 2] += t[2]; o[4 * i + 3] += t[3]; }
    auto lr = __builtin_amdgcn_permlane32_swap(__float_as_uint(o[8]), __float_as_uint(o[8]), false, false);
    float l = __uint_as_float(lr[1]);
    bool ok = (l >= 0x1p-110f) && (l <= 0x1p100f);
#pragma unroll
    for (int i = 0; i < 12; ++i) ok = ok && (fabsf(o[i]) <= 0x1p126f || (h && i >= 8));
    if (__builtin_expect(!__all(ok), 0)) {
        o = attn_fallback(ws, s.qa0, s.qa1, lane);
        lr = __builtin_amdgcn_permlane32_swap(__float_as_uint(o[8]), __float_as_uint(o[8]), false, false);
        l = __uint_as_float(lr[1]);
    }
    const float rl = 1.f / l;
    lds_ptr ob = lds + L_OUT + w * 1280;
    *(LDSP u32x2*)(ob + r31 * 40 + h * 8) = u32x2{pk2(o[0] * rl, o[1] * rl), pk2(o[2] * rl, o[3] * rl)};
    *(LDSP u32x2*)(ob + r31 * 40 + h * 8 + 16) = u32x2{pk2(o[4] * rl, o[5] * rl), pk2(o[6] * rl, o[7] * rl)};
    if (!h) *(LDSP u32x2*)(ob + r31 * 40 + 32) = u32x2{pk2(o[8] * rl, o[9] * rl), pk2(o[10] * rl, o[11] * rl)};
    asm volatile("s_waitcnt lgkmcnt(0)" ::: "memory");
    const rsrc_t rou = mk_rsrc(ws + OFF_OU, S_ * 3072);
    st128(rou, (unsigned)((tA + hf) * 3072 + lane * 16), ldsr128(ob + lane * 16));
    if (lane < 16) st128(rou, (unsigned)((tA + hf) * 3072 + 1024 + lane * 16), ldsr128(ob + 1024 + lane * 16));
}

DI void zload(u32x4& z, rsrc_t r, unsigned voff, unsigned soff) {
    asm volatile("buffer_load_dwordx4 %0, %1, %2, %3 offen sc1" : "+v"(z) : "v"(voff), "s"(r), "s"(soff) : "memory");
}
#define K2_GSPLIT 1
#define K2_PFSS 6
#define K2_POLL 16
#define GBURST_PRIO 2
#define GSTEP_PRIO 3
struct Gs {
    rsrc_t rkv, rw, rz; unsigned vkv, vw[8], skv[6], sw, wstr, dkv, dw, zoff;
    u32x4 z[16]; f32x16 g0, g1; u32x4 wa0, wa1;
};
template <int I> DI void kv_dma(const Gs& g) { bdma(g.rkv, g.vkv, g.skv[I], g.dkv + I * 1024); }
template <int I> DI void w_dma(const Gs& g) { bdma(g.rw, g.vw[I], g.sw + I * g.wstr, g.dw + I * 1024); }
DI void z_loads(Gs& g, unsigned zs) {
#pragma unroll
    for (int i = 0; i < 16; ++i) zload(g.z[i], g.rz, g.zoff, zs + i * ZSTEP);
}
template <int I> DI void kv_all(const Gs& g) { kv_dma<I>(g); if constexpr (I < 5) kv_all<I + 1>(g); }
template <int I> DI void w_all(const Gs& g) { w_dma<I>(g); if constexpr (I < 7) w_all<I + 1>(g); }
#define Z_TIE(g) "+v"(g.z[0]), "+v"(g.z[1]), "+v"(g.z[2]), "+v"(g.z[3]), "+v"(g.z[4]), "+v"(g.z[5]), "+v"(g.z[6]), "+v"(g.z[7]), \
                 "+v"(g.z[8]), "+v"(g.z[9]), "+v"(g.z[10]), "+v"(g.z[11]), "+v"(g.z[12]), "+v"(g.z[13]), "+v"(g.z[14]), "+v"(g.z[15])
template <int I> DI void g_burst(Gs& g, lds_ptr wp, unsigned wx) {
    u32x4 wn0, wn1;
    if constexpr (I == 0) { g.wa0 = ldsr128(wp + wx); g.wa1 = ldsr128(wp + wx + 16384); }
    if constexpr (I < 15) { wn0 = ldsr128(wp + (wx ^ ((I + 1) * 32))); wn1 = ldsr128(wp + (wx ^ ((I + 1) * 32)) + 16384); }
    g.g0 = mfma32(g.z[I], g.wa0, g.g0); g.g1 = mfma32(g.z[I], g.wa1, g.g1);
    if constexpr (I < 15) { g.wa0 = wn0; g.wa1 = wn1; }
    SBAR();
    if constexpr (I < 15) g_burst<I + 1>(g, wp, wx);
}
template <int J> DI void dma_piece(const Gs& g) { if constexpr (J < 8) w_dma<J>(g); else kv_dma<J - 8>(g); }
template <int N> DI void z_wait(u32x4& z) { asm volatile("s_waitcnt vmcnt(%1)" : "+v"(z) : "i"(N) : "memory"); }
template <int I, bool FIRST> DI void g_step(Gs& g, lds_ptr wp, unsigned wx, unsigned zs) {
    u32x4 wn0, wn1;
    if constexpr (I == 0) { g.wa0 = ldsr128(wp + wx); g.wa1 = ldsr128(wp + wx + 16384); }
    if constexpr (I < 15) { wn0 = ldsr128(wp + (wx ^ ((I + 1) * 32))); wn1 = ldsr128(wp + (wx ^ ((I + 1) * 32)) + 16384); }
    constexpr int nd = I < 3 ? 2 : I < 11 ? 1 : 0;
    constexpr int before = I <= 3 ? 2 * I : I <= 11 ? I + 3 : 14;
    z_wait<FIRST ? 15 + before : 29 - nd>(g.z[I]);
    g.g0 = mfma32(g.z[I], g.wa0, g.g0);
    SBAR();
    if constexpr (I < 3) { dma_piece<2 * I>(g); dma_piece<2 * I + 1>(g); } else if constexpr (I < 11) dma_piece<I + 3>(g);
    else __builtin_amdgcn_s_sleep(1);
    SBAR();
    g.g1 = mfma32(g.z[I], g.wa1, g.g1);
    zload(g.z[I], g.rz, g.zoff, zs + I * ZSTEP);
    if constexpr (I < 15) { g.wa0 = wn0; g.wa1 = wn1; }
    SBAR();
    if constexpr (I < 15) g_step<I + 1, FIRST>(g, wp, wx, zs);
}
template <bool HASG>
DI void gating(const Params& P, lds_ptr lds, unsigned lds0, int b, int gw, int lane, int t0, int f0 STAMP_ARG) {
    char* ws = P.ws;
    const int t256 = gw * 64 + lane, r31 = lane & 31, h = lane >> 5;
    __builtin_amdgcn_s_setprio(3);
    const char* zr0 = ws + OFF_Z2R + (unsigned)(b * FH_ * 16);
    u32x4 c[4];
#pragma unroll
    for (int i = 0; i < 4; ++i) c[i] = *(const u32x4*)(zr0 + (i < 3 || t256 < FH_ - 768 ? t256 + 256 * i : t256) * 16);
    if (gw == 0 && lane < 32) ((LDSP unsigned*)(lds + L_RED))[64 + lane] = 0u;
    BAR_LDS();
    STAMP(21);
    USE(c[0][0]); USE(c[1][0]); USE(c[2][0]); USE(c[3][0]);
    STAMP(22);
    if (t256 >= FH_ - 768) c[3] = u32x4{0, 0, 0, 0};
    LDSP float* red = (LDSP float*)(lds + L_RED);
    {
        float v[16];
#pragma unroll
        for (int j = 0; j < 8; ++j) { v[j] = 0.f; v[8 + j] = 0.f; }
#pragma unroll
        for (int i = 0; i < 4; ++i)
#pragma unroll
            for (int j = 0; j < 4; ++j) {
                const float lo = bflo(c[i][j]), hi = bfhi(c[i][j]);
                v[2 * j] += lo; v[2 * j + 1] += hi; v[8 + 2 * j] = __builtin_fmaf(lo, lo, v[8 + 2 * j]); v[8 + 2 * j + 1] = __builtin_fmaf(hi, hi, v[8 + 2 * j + 1]);
            }
        float u[8], t[4];
#pragma unroll
        for (int j = 0; j < 8; ++j) { auto r = __builtin_amdgcn_permlane32_swap(__float_as_uint(v[j]), __float_as_uint(v[j + 8]), false, false); u[j] = __uint_as_float(r[0]) + __uint_as_float(r[1]); }
#pragma unroll
        for (int j = 0; j < 4; ++j) { auto r = __builtin_amdgcn_permlane16_swap(__float_as_uint(u[j]), __float_as_uint(u[j + 4]), false, false); t[j] = __uint_as_float(r[0]) + __uint_as_float(r[1]); }
#pragma unroll
        for (int j = 0; j < 4; ++j) {
            t[j] += __uint_as_float(__builtin_amdgcn_update_dpp(0, __float_as_uint(t[j]), 0x128, 0xf, 0xf, false));
            t[j] += __uint_as_float(__builtin_amdgcn_update_dpp(0, __float_as_uint(t[j]), 0x124, 0xf, 0xf, false));
            t[j] += __uint_as_float(__builtin_amdgcn_update_dpp(0, __float_as_uint(t[j]), 0x122, 0xf, 0xf, false));
            t[j] += __uint_as_float(__builtin_amdgcn_update_dpp(0, __float_as_uint(t[j]), 0x121, 0xf, 0xf, false));
        }
        if ((lane & 15) == 0) { const int rho = lane >> 4; *(LDSP f32x4*)(red + gw * 16 + (rho >> 1) * 8 + (rho & 1) * 4) = f32x4{t[0], t[1], t[2], t[3]}; }
        asm volatile("s_waitcnt lgkmcnt(0)" ::: "memory");
        lds_add(lds, C_NORM);
        STAMP(2);
        lds_wait_ge(lds, C_NORM, 4u);
        float sm = 0, sq = 0;
#pragma unroll
        for (int j = 0; j < 4; ++j) { sm += red[j * 16 + (lane & 7)]; sq += red[j * 16 + 8 + (lane & 7)]; }
        const float mu = sm * (1.f / FH_), var = fmaxf(sq * (1.f / FH_) - mu * mu, 0.f), rs = __builtin_amdgcn_rsqf(var + 1e-5f), sh = -mu * rs;
        float a[8], bb[8];
#pragma unroll
        for (int j = 0; j < 8; ++j) { a[j] = __uint_as_float(__builtin_amdgcn_readlane(__float_as_uint(rs), j)); bb[j] = __uint_as_float(__builtin_amdgcn_readlane(__float_as_uint(sh), j)); }
        const rsrc_t zw = mk_rsrc(ws + OFF_Z2N + (unsigned)(b * FH_ * 16), FH_ * 16);
#pragma unroll
        for (int i = 0; i < 4; ++i) {
            u32x4 o;
#pragma unroll
            for (int j = 0; j < 4; ++j) o[j] = pk2(__builtin_fmaf(bflo(c[i][j]), a[2 * j], bb[2 * j]), __builtin_fmaf(bfhi(c[i][j]), a[2 * j + 1], bb[2 * j + 1]));
            __builtin_amdgcn_raw_buffer_store_b128(o, zw, (t256 + 256 * i) * 16, 0, 16);
        }
    }
    STAMP(23);
    Gs g;
    g.rkv = mk_rsrc(ws + OFF_KF, (unsigned)(OFF_Z1 - OFF_KF)); g.rw = mk_rsrc(P.wsp, S_ * 4096); g.rz = mk_rsrc(ws + OFF_Z2N, (unsigned)(S_ * FH_ * 2 + 4096));
    g.vkv = lane * 16;
#pragma unroll
    for (int i = 0; i < 8; ++i) g.vw[i] = (lane >> 5) * 4096 + (((lane & 31) ^ ((2 * i + (lane >> 5)) & 15)) << 4);
    const int rot = 0;
    const unsigned sw0 = (t0 + 16 * gw) * 4096;
    g.wstr = 2 * 4096;
    g.sw = sw0 + rot * 512;
    const unsigned wdst = lds0 + L_W + 8 * gw * 1024;
    if (HASG) { g.dw = wdst; w_all<0>(g); asm volatile("s_waitcnt vmcnt(8)" ::: "memory"); }
    else WAIT_VM(0);
    unsigned* flags = (unsigned*)(ws + OFF_FLAG);
    if (lane == 0) {
        const unsigned old = __hip_atomic_fetch_add((LDSP unsigned*)(lds + L_RED) + C_PUB, 1u, __ATOMIC_RELAXED, __HIP_MEMORY_SCOPE_WORKGROUP);
        if (old == 3u) __hip_atomic_store(flags + (b >> 2) * 32 + (b & 3), 1u, __ATOMIC_RELAXED, __HIP_MEMORY_SCOPE_AGENT);
    }
    STAMP(5);
    const int ph = gw >> 1;
    const unsigned kvinc = (gw & 1) ? 4 * VT_BYTES : 4 * KT_BYTES;
    {
        const unsigned base = ((gw & 1) ? (unsigned)(OFF_VF - OFF_KF) + (ph * 32) * VT_BYTES : (ph * 32 + 1) * KT_BYTES) + 3 * kvinc;
#pragma unroll
        for (int i = 0; i < 6; ++i) g.skv[i] = base + i * 1024;
    }
    const unsigned kvdst = lds0 + L_KV + 6 * gw * 1024;
    if constexpr (!HASG) {
        lds_add(lds, C_GAT);
        int buf = 0;
#pragma unroll 1
        for (int m = 0; m < 5; ++m) {
            lds_wait_ge(lds, C_ATT + m, 8u);
            g.dkv = kvdst + buf * KV_STAGE;
            kv_all<0>(g);
#pragma unroll
            for (int i = 0; i < 6; ++i) g.skv[i] += kvinc;
            WAIT_VM(0);
            lds_add(lds, C_GAT);
            buf = (buf == 2) ? 0 : buf + 1;
        }
        lds_wait_ge(lds, C_ATT + K2_PFSS, 8u);
        {
            const int part = ((b >> 3) - 28) * 4 + gw;
            const unsigned colb = (lane % 10) * 128u, rsub = min(lane / 10, 5);
            const rsrc_t rr = part < 7 ? mk_rsrc(P.wo, D_ * 1280) : mk_rsrc(P.wout, FH_ * 1280);
            const unsigned r0 = (unsigned)((part < 7 ? part : part - 7) * 96) * 1280;
            touch_lines8(rr, rsub * 1280 + colb, r0, 6 * 1280);
            touch_lines8(rr, rsub * 1280 + colb, r0 + 48 * 1280, 6 * 1280);
            touch_lines8(mk_rsrc(P.x, S_ * 1280), min(rsub, 1u) * 1280 + colb, (unsigned)((b & 7) * 256 + part * 16) * 1280, 2 * 1280);
        }
        return;
    }
    g.zoff = (h * FH_ + f0 + r31) * 16;
    g.g0 = f32x16{}; g.g1 = f32x16{};
#pragma unroll
    for (int i = 0; i < 16; ++i) g.z[i] = u32x4{0, 0, 0, 0};
    lds_ptr wb = lds + L_W;
    const unsigned wx = r31 * 512 + (((lane & 15) ^ h) << 4);
    if (gw == 0) {
        const u32x4* fp = (const u32x4*)flags + lane * 8;
        for (unsigned spin = 0; spin < (1u << 22); ++spin) {
            __builtin_amdgcn_s_sleep(K2_POLL);
            u32x4 f;
            asm volatile("global_load_dwordx4 %0, %1, off sc1\n\ts_waitcnt vmcnt(0)" : "=&v"(f) : "v"(fp) : "memory");
            if (__all(f[0] == 1u && f[1] == 1u && f[2] == 1u && f[3] == 1u)) break;
        }
        lds_add(lds, C_FLAG);
    } else lds_wait_ge(lds, C_FLAG, 1u);
    STAMP(6);
    WAIT_VM(0);
    lds_add(lds, C_GAT);
    __builtin_amdgcn_s_setprio(GSTEP_PRIO);
    z_loads(g, rot * 16 * ZSTEP);
    int buf = 0;
    auto stage_head = [&](int m) {
        lds_wait_ge(lds, C_GAT, 4u * (m + 1));
        if (m < 5) lds_wait_ge(lds, C_ATT + m, 8u);
        if (m == 5) g.rkv = mk_rsrc(ws, 0);
        g.dkv = (m < 5) ? kvdst + buf * KV_STAGE : lds0 + L_DUMMY; g.dw = wdst + ((m + 1) & 1) * W_STAGE;
        g.sw = sw0 + ((m + 1 + rot) & 7) * 512;
        if (m == 7) {
            g.rz = mk_rsrc(ws, 0);
            g.rw = mk_rsrc(ws + OFF_Z1, S_ * 1728); g.sw = (t0 + 16 * gw) * 1728 + (f0 >> 7) * 256; g.wstr = 4 * 1728;
#pragma unroll
            for (int i = 0; i < 8; ++i) g.vw[i] = i < 4 ? (lane >> 4) * 1728 + (((lane & 15) ^ ((4 * i + (lane >> 4)) & 15)) << 4) : 0x7f000000u;
        }
    };
    auto stage_tail = [&]() {
#pragma unroll
        for (int i = 0; i < 6; ++i) g.skv[i] += kvinc;
        asm volatile("s_waitcnt vmcnt(6) lgkmcnt(0)" ::: "memory");
        lds_add(lds, C_GAT);
        buf = (buf == 2) ? 0 : buf + 1;
    };
    stage_head(0); g_step<0, true>(g, wb, wx, ((1 + rot) & 7) * 16 * ZSTEP); stage_tail();
#pragma unroll 1
    for (int m = 1; m < 8; ++m) { stage_head(m); g_step<0, false>(g, wb + (m & 1) * W_STAGE, wx, ((m + 1 + rot) & 7) * 16 * ZSTEP); stage_tail(); }
    asm volatile("s_waitcnt vmcnt(0)" : Z_TIE(g) :: "memory");
    STAMP(15);
    lds_wait_ge(lds, C_GAT, 36u);
#pragma unroll
    for (int u = 0; u < 2; ++u) {
        const int r = 32 * u + r31;
        const float bias = bf2f(P.bsp[t0 + r]);
        const f32x16& gg = u ? g.g1 : g.g0;
#pragma unroll
        for (int i = 0; i < 4; ++i) {
            const int ch = (4 * gw + i) ^ (r & 15);
            u32x2 o = u32x2{0u, 0u};
            if (f0 < FH_) {
                const u32x2 z1 = *(const LDSP u32x2*)(lds + L_W + (r >> 4) * 8192 + (r & 15) * 256 + ch * 16 + h * 8);
                o = u32x2{pk2(bflo(z1[0]) * (gg[4 * i] + bias), bfhi(z1[0]) * (gg[4 * i + 1] + bias)),
                          pk2(bflo(z1[1]) * (gg[4 * i + 2] + bias), bfhi(z1[1]) * (gg[4 * i + 3] + bias))};
            }
            *(LDSP u32x2*)(lds + L_W + W_STAGE + r * 256 + ch * 16 + h * 8) = o;
        }
    }
    asm volatile("s_waitcnt lgkmcnt(0)" ::: "memory");
    lds_add(lds, C_UST);
    lds_wait_ge(lds, C_UST, 4u);
    const rsrc_t rou = mk_rsrc(ws + OFF_OU, S_ * 3072);
#pragma unroll
    for (int i = 0; i < 4; ++i) {
        const int r = 16 * gw + 4 * i + (lane >> 4), c = lane & 15;
        st128(rou, (unsigned)((t0 + r) * 3072 + 1280 + (f0 >> 7) * 256 + c * 16), ldsr128(lds + L_W + W_STAGE + r * 256 + ((c ^ (r & 15)) << 4)));
    }
}

__global__ __launch_bounds__(768, 3) void k2_mix(Params P) {
    extern __shared__ __attribute__((aligned(16))) char smem[];
    const int lane = threadIdx.x & 63, w = __builtin_amdgcn_readfirstlane(threadIdx.x >> 6);
    STAMP_DECL; STAMP(0); RSTAMP(30);
    const int b = blockIdx.x, j = b >> 3;
    lds_ptr lds = (lds_ptr)smem; const unsigned lds0 = (unsigned)(uintptr_t)smem;
    if (w < 8) attention(P, lds, lds0, b, w, lane STAMP_PASS);
    else if (j < 28) { const int rbg = (b & 7) * 4 + j / 7, cbg = j % 7; gating<true>(P, lds, lds0, b, w - 8, lane, rbg * 64, cbg * 128 + (w - 8) * 32 STAMP_PASS); }
    else gating<false>(P, lds, lds0, b, w - 8, lane, 0, 0 STAMP_PASS);
    STAMP(17); RSTAMP(31); STAMP_FLUSH_K(1);
}
}
namespace k3 {
#define K3_AUX 16
constexpr int BM = 96, A_BYTES = BM * 128, STAGE = 20480, NST = 7, NKS = 24, LDS_BYTES = NST * STAGE;
struct Params { const u16* x; const u16* wo; const u16* wout; const char* ws; u16* out; };
struct Ld { rsrc_t ra, rb0, rb1, rx; unsigned va[2], vb[2], sa, sb, da, db, vx, sx; int rot; };
constexpr int L_X = 3 * STAGE;
template <int S> DI void ld_issue(const Ld& L) {
    constexpr int so = (S % NST) * STAGE;
    int gs = S + L.rot; if (gs >= NKS) gs -= NKS;
    const bool lo = gs < 10;
    const rsrc_t rb = lo ? L.rb0 : L.rb1;
    const unsigned sb = L.sb + (unsigned)(lo ? gs : gs - 10) * (64 * 1280);
#pragma unroll
    for (int j = 0; j < 3; ++j) bdma(L.ra, L.va[j & 1], L.sa + gs * 128 + j * 8 * 3072, L.da + so + j * 1024);
#pragma unroll
    for (int j = 0; j < 2; ++j) bdma(rb, L.vb[j], sb + j * 8 * 1280, L.db + so + j * 1024);
}
constexpr int issued_at(int S) { return (S < 3 ? 2 + 2 * S : S + 5) < NKS ? (S < 3 ? 2 + 2 * S : S + 5) : NKS - 1; }
template <int A, int B> DI void ld_range(const Ld& L) { if constexpr (A <= B) { ld_issue<A>(L); ld_range<A + 1, B>(L); } }
template <int S> DI void ld_step(const Ld& L) {
    if constexpr (S > 0) ld_range<issued_at(S - 1) + 1, issued_at(S)>(L);
    if constexpr (S == 19) {
#pragma unroll
        for (int j = 0; j < 3; ++j) bdma(L.rx, L.vx, L.sx + j * 8 * 1280, L.da + L_X + j * 1024);
    }
    constexpr int young = 5 * (issued_at(S) - (S + 1)) + (S >= 19 && S < NKS - 2 ? 3 : 0);
    asm volatile("s_waitcnt vmcnt(%0) lgkmcnt(0)\n\ts_barrier" :: "i"(young) : "memory");
    if constexpr (S + 2 < NKS) ld_step<S + 1>(L);
}
struct Frag { u32x4 x[3], w[2]; };
struct Ctx { unsigned xo[3], wo0[3], wo1[3]; };
template <int S> DI void rd(Frag& f, lds_ptr lds, const Ctx& c) {
    constexpr int slot = S % NST, grp = slot / 3, so = (slot % 3) * STAGE;
#pragma unroll
    for (int n = 0; n < 2; ++n) {
        lds_ptr wp = lds + (n ? c.wo1[grp] : c.wo0[grp]) + so;
        const u32x2 lo = ldstr(wp), hi = ldstr(wp + 512);
        f.w[n] = u32x4{lo[0], lo[1], hi[0], hi[1]};
    }
#pragma unroll
    for (int m = 0; m < 3; ++m) f.x[m] = ldsr128(lds + c.xo[grp] + so + m * 2048);
}
template <int S> DI void stage(lds_ptr lds, const Ctx& c, Frag& fc, Frag& fn, f32x4 (&acc)[3][2] STAMP_ARG) {
    if constexpr (S + 1 < NKS) {
        BAR_LDS();
        STAMP(2 + S);
        rd<S + 1>(fn, lds, c);
        SBAR();
    }
#pragma unroll
    for (int m = 0; m < 3; ++m)
#pragma unroll
        for (int n = 0; n < 2; ++n) acc[m][n] = mfma16(fc.w[n], fc.x[m], acc[m][n]);
    SBAR();
    if constexpr (S + 1 < NKS) stage<S + 1>(lds, c, fn, fc, acc STAMP_PASS);
}

__global__ __launch_bounds__(768, 3) void k3_out(Params P) {
    extern __shared__ __attribute__((aligned(16))) char smem[];
    const int tid = threadIdx.x, lane = tid & 63, w = __builtin_amdgcn_readfirstlane(tid >> 6);
    STAMP_DECL; STAMP(0); RSTAMP(30);
    const int b = blockIdx.x, j0 = b >> 3;
    if (j0 >= 30) return;
    const int rbl = j0 / 10, cb = j0 - rbl * 10;
    const int row0 = (b & 7) * 256 + rbl * BM, rowlim = (b & 7) * 256 + 256;
    lds_ptr lds = (lds_ptr)smem;
    const unsigned lds0 = (unsigned)(uintptr_t)smem;
    if (w >= 8) {
        const int L = w - 8, r8 = lane >> 3, c8 = lane & 7, k4 = lane >> 4;
        Ld ld;
        ld.ra = mk_rsrc(P.ws + OFF_OU, rowlim * 3072); ld.rb0 = mk_rsrc(P.wo, D_ * 1280); ld.rb1 = mk_rsrc(P.wout, FH_ * 1280);
#pragma unroll
        for (int i = 0; i < 2; ++i) {
            ld.va[i] = r8 * 3072 + ((c8 ^ ((4 * ((L + i) & 1) + k4) & 7)) << 4);
            ld.vb[i] = r8 * 1280 + ((c8 ^ (((k4 & 1) | (i << 1)) << 1)) << 4);
        }
        ld.sa = (row0 + 24 * L) * 3072; ld.sb = cb * 128 + 16 * L * 1280;
        ld.da = lds0 + 3 * L * 1024; ld.db = lds0 + A_BYTES + 2 * L * 1024;
        ld.rx = mk_rsrc(P.x, rowlim * 1280); ld.vx = r8 * 1280 + c8 * 16; ld.sx = (row0 + 24 * L) * 1280 + cb * 128;
        ld.rot = 0;
        STAMP(1);
        ld_issue<0>(ld); ld_issue<1>(ld); ld_issue<2>(ld);
        STAMP(2);
        WAIT_BAR(10);
        STAMP(3);
        ld_step<0>(ld);
        return;
    }
    const int kh = w >> 2, wr = w & 1, wc = (w >> 1) & 1;
    const int r15 = lane & 15, g = lane >> 4, q = r15 >> 2, p = lane & 3;
    Ctx c;
    const unsigned xoff = (48 * wr + r15) * 128 + (((4 * kh + g) ^ ((r15 >> 1) & 7)) << 4);
    const int fk = (((q >> 1) & 1) | ((g & 1) << 1)) << 1;
    const unsigned woff = A_BYTES + (32 * kh + 8 * g + q) * 128 + (((4 * wc + (p >> 1)) ^ fk) << 4) + 8 * (p & 1);
#pragma unroll
    for (int i = 0; i < 3; ++i) { c.xo[i] = xoff + i * 3 * STAGE; c.wo0[i] = woff + i * 3 * STAGE; c.wo1[i] = (woff ^ 32) + i * 3 * STAGE; }
    f32x4 acc[3][2];
#pragma unroll
    for (int m = 0; m < 3; ++m) { acc[m][0] = f32x4{0, 0, 0, 0}; acc[m][1] = f32x4{0, 0, 0, 0}; }
    Frag f0, f1;
    BAR_LDS();
    rd<0>(f0, lds, c);
    const int co = cb * 64 + wc * 32;

    stage<0>(lds, c, f0, f1, acc STAMP_PASS);
    STAMP(28);
    BAR_LDS();
    STAMP(25);
    LDSP f32x4* xch = (LDSP f32x4*)lds + (w & 3) * 6 * 64;
    if (kh == 1) {
#pragma unroll
        for (int m = 0; m < 2; ++m) { xch[(m * 2) * 64 + lane] = acc[m][0]; xch[(m * 2 + 1) * 64 + lane] = acc[m][1]; }
    } else { xch[4 * 64 + lane] = acc[2][0]; xch[5 * 64 + lane] = acc[2][1]; }
    BAR_LDS();
    STAMP(26);
#pragma unroll
    for (int u = 0; u < 2; ++u) {
        if (kh == 1 && u == 1) break;
        const int m = kh ? 2 : u;
        const int tb = row0 + 48 * wr + 16 * m;
        if (tb >= rowlim) break;
        const int t = tb + r15;
        unsigned pk[2][2];
#pragma unroll
        for (int n = 0; n < 2; ++n) {
            const f32x4 mine = kh ? acc[2][n] : acc[u][n];
            const f32x4 v = mine + xch[(m * 2 + n) * 64 + lane];
            const u32x2 xv = *(const LDSP u32x2*)(lds + L_X + (48 * wr + 16 * m + r15) * 128 + (wc * 32 + 16 * n + 4 * g) * 2);
            pk[n][0] = pk2(v[0] + bflo(xv[0]), v[1] + bfhi(xv[0])); pk[n][1] = pk2(v[2] + bflo(xv[1]), v[3] + bfhi(xv[1]));
        }
        auto x0 = __builtin_amdgcn_permlane16_swap(pk[0][0], pk[1][0], false, false);
        auto x1 = __builtin_amdgcn_permlane16_swap(pk[0][1], pk[1][1], false, false);
        __builtin_amdgcn_raw_buffer_store_b128(u32x4{x0[0], x1[0], x0[1], x1[1]}, mk_rsrc(P.out, S_ * 1280), (unsigned)(t * D_ + co + 16 * (g & 1) + 8 * (g >> 1)) * 2, 0, K3_AUX);
    }
    STAMP(29); RSTAMP(31); STAMP_FLUSH_K(2);
}
}
extern "C" void kernel_launch(void* const* d_in, const int* in_sizes, int n_in, void* d_out, int out_size, void* d_ws, size_t ws_size,
                              hipStream_t stream) {
    (void)in_sizes; (void)n_in; (void)out_size; (void)ws_size;
    const u16* x = (const u16*)d_in[0];
    k1::Params p1{x, (const u16*)d_in[1], (const u16*)d_in[2], (const u16*)d_in[3], (const u16*)d_in[6], (const u16*)d_in[7], (char*)d_ws};
    k2::Params p2{(const u16*)d_in[7], (const u16*)d_in[8], (const u16*)d_in[4], (const u16*)d_in[9], x, (char*)d_ws};
    k3::Params p3{x, (const u16*)d_in[4], (const u16*)d_in[9], (const char*)d_ws, (u16*)d_out};
    (void)hipFuncSetAttribute((const void*)k1::k1_proj, hipFuncAttributeMaxDynamicSharedMemorySize, k1::LDS_BYTES);
    (void)hipFuncSetAttribute((const void*)k2::k2_mix, hipFuncAttributeMaxDynamicSharedMemorySize, k2::LDS_BYTES);
    (void)hipFuncSetAttribute((const void*)k3::k3_out, hipFuncAttributeMaxDynamicSharedMemorySize, k3::LDS_BYTES);
    k1::k1_proj<<<256, 768, k1::LDS_BYTES, stream>>>(p1);
    k2::k2_mix<<<256, 768, k2::LDS_BYTES, stream>>>(p2);
    k3::k3_out<<<256, 768, k3::LDS_BYTES, stream>>>(p3);
}
```

```cpp
#include <hip/hip_runtime.h>
#include <stdint.h>
#pragma clang diagnostic ignored "-Winline-asm"

typedef unsigned short u16;
using bf16x8 = __attribute__((ext_vector_type(8))) __bf16;
using bf16x2 = __attribute__((ext_vector_type(2))) __bf16;
using s16x4 = __attribute__((ext_vector_type(4))) short;
using f32x2 = __attribute__((ext_vector_type(2))) float;
using f32x4 = __attribute__((ext_vector_type(4))) float;
using f32x16 = __attribute__((ext_vector_type(16))) float;
using u32x2 = __attribute__((ext_vector_type(2))) unsigned;
using u32x4 = __attribute__((ext_vector_type(4))) unsigned;
#define DI __device__ __forceinline__
#define LDSP __attribute__((address_space(3)))
typedef LDSP char* lds_ptr;

constexpr int S_ = 2048, D_ = 640, FH_ = 864, F_ = 1728;
constexpr size_t OFF_Q = 0;
constexpr size_t OFF_KF = OFF_Q + (size_t)S_ * D_ * 2;
constexpr size_t OFF_VF = OFF_KF + 131072;
constexpr size_t OFF_Z1 = OFF_VF + 131072;
constexpr size_t OFF_Z2R = OFF_Z1 + (size_t)S_ * FH_ * 2;
constexpr size_t OFF_Z2N = OFF_Z2R + (size_t)S_ * FH_ * 2;
constexpr size_t OFF_OU = OFF_Z2N + (size_t)S_ * FH_ * 2 + 4096;
constexpr size_t OFF_FLAG = OFF_OU + (size_t)S_ * 1536 * 2;
constexpr int V_SLOTS = 22;
constexpr int KT_BYTES = 1536, VT_BYTES = 4 * V_SLOTS * 16;

DI float bf2f(u16 b) { return __uint_as_float(((unsigned)b) << 16); }
DI unsigned pk2(float lo, float hi) { bf16x2 v = {(__bf16)lo, (__bf16)hi}; return __builtin_bit_cast(unsigned, v); }
DI float bflo(unsigned v) { return __uint_as_float(v << 16); }
DI float bfhi(unsigned v) { return __uint_as_float(v & 0xffff0000u); }
DI void glds16(const void* g, unsigned lds_base) {
    unsigned sv;
    asm volatile("s_mov_b32 %0, m0\n\ts_mov_b32 m0, %2\n\ts_nop 0\n\tglobal_load_lds_dwordx4 %1, off\n\ts_mov_b32 m0, %0"
                 : "=&s"(sv) : "v"(g), "s"(lds_base) : "memory");
}
DI void glds(const void* g, unsigned lds_dst) {
    asm volatile("s_mov_b32 m0, %1\n\ts_nop 0\n\tglobal_load_lds_dwordx4 %0, off" :: "v"(g), "s"(lds_dst) : "memory", "m0");
}
DI void glds_lo32(const void* g, unsigned lds_dst) {
    asm volatile("s_mov_b32 m0, %1\n\ts_mov_b32 exec_hi, 0\n\tglobal_load_lds_dwordx4 %0, off\n\ts_mov_b32 exec_hi, -1" :: "v"(g), "s"(lds_dst) : "memory", "m0");
}
typedef __amdgpu_buffer_rsrc_t rsrc_t;
DI rsrc_t mk_rsrc(const void* p, unsigned bytes) { return __builtin_amdgcn_make_buffer_rsrc((void*)p, 0, bytes, 0x00020000); }
DI void bdma(rsrc_t r, unsigned voff, unsigned soff, unsigned lds_dst) {
    asm volatile("s_mov_b32 m0, %3\n\ts_nop 0\n\tbuffer_load_dwordx4 %0, %1, %2 offen lds" :: "v"(voff), "s"(r), "s"(soff), "s"(lds_dst) : "memory", "m0");
}
#define WAIT_VM(n) asm volatile("s_waitcnt vmcnt(" #n ")" ::: "memory")
#define WAIT_BAR(n) asm volatile("s_waitcnt vmcnt(" #n ") lgkmcnt(0)\n\ts_barrier" ::: "memory")
#define BAR_LDS() asm volatile("s_waitcnt lgkmcnt(0)\n\ts_barrier" ::: "memory")
#define SBAR() __builtin_amdgcn_sched_barrier(0)
DI u32x4 ldsr128(lds_ptr p) { return *(const LDSP u32x4*)p; }
typedef short v4i16_t __attribute__((ext_vector_type(4)));
DI u32x2 ldstr(lds_ptr p) { return __builtin_bit_cast(u32x2, __builtin_amdgcn_ds_read_tr16_b64_v4i16((LDSP v4i16_t*)p)); }
#define ST_AUX 16
DI void st128(rsrc_t r, unsigned off, u32x4 v) { __builtin_amdgcn_raw_buffer_store_b128(v, r, off, 0, ST_AUX); }
DI void st64(rsrc_t r, unsigned off, u32x2 v) { __builtin_amdgcn_raw_buffer_store_b64(v, r, off, 0, ST_AUX); }
DI void touch_lines8(rsrc_t r, unsigned voff, unsigned soff, unsigned step) {
    unsigned t0, t1, t2, t3, t4, t5, t6, t7;
    asm volatile("buffer_load_dword %0, %8, %9, %10 offen\n\tbuffer_load_dword %1, %8, %9, %11 offen\n\tbuffer_load_dword %2, %8, %9, %12 offen\n\t"
                 "buffer_load_dword %3, %8, %9, %13 offen\n\tbuffer_load_dword %4, %8, %9, %14 offen\n\tbuffer_load_dword %5, %8, %9, %15 offen\n\t"
                 "buffer_load_dword %6, %8, %9, %16 offen\n\tbuffer_load_dword %7, %8, %9, %17 offen\n\ts_waitcnt vmcnt(0)"
                 : "=&v"(t0), "=&v"(t1), "=&v"(t2), "=&v"(t3), "=&v"(t4), "=&v"(t5), "=&v"(t6), "=&v"(t7)
                 : "v"(voff), "s"(r), "s"(soff), "s"(soff + step), "s"(soff + 2 * step), "s"(soff + 3 * step), "s"(soff + 4 * step), "s"(soff + 5 * step),
                   "s"(soff + 6 * step), "s"(soff + 7 * step) : "memory");
}
DI void touch_line(unsigned& t, rsrc_t r, unsigned voff, unsigned soff) {
    asm volatile("buffer_load_dword %0, %1, %2, %3 offen" : "=v"(t) : "v"(voff), "s"(r), "s"(soff) : "memory");
}
#define TOUCH_DONE(t) asm volatile("" :: "v"(t))
DI f32x4 mfma16(u32x4 a, u32x4 b, f32x4 c) { return __builtin_amdgcn_mfma_f32_16x16x32_bf16(__builtin_bit_cast(bf16x8, a), __builtin_bit_cast(bf16x8, b), c, 0, 0, 0); }
DI void mfma16_acc(f32x4& acc, const u32x4& a, const u32x4& b) { asm volatile("v_mfma_f32_16x16x32_bf16 %0, %1, %2, %0" : "+v"(acc) : "v"(a), "v"(b)); }
DI f32x16 mfma32(u32x4 a, u32x4 b, f32x16 c) { return __builtin_amdgcn_mfma_f32_32x32x16_bf16(__builtin_bit_cast(bf16x8, a), __builtin_bit_cast(bf16x8, b), c, 0, 0, 0); }
DI f32x16 mfma32k8(u32x2 a, u32x2 b, f32x16 c) { return __builtin_amdgcn_mfma_f32_32x32x8bf16_1k(__builtin_bit_cast(s16x4, a), __builtin_bit_cast(s16x4, b), c, 0, 0, 0); }
DI float dot2(unsigned a, unsigned b, float c) { return __builtin_amdgcn_fdot2_f32_bf16(__builtin_bit_cast(bf16x2, a), __builtin_bit_cast(bf16x2, b), c, false); }
DI float gelu_tanh(float x) {
    const float c0 = -2.302208198f, c1 = -0.1029432397f;
    float e = __builtin_amdgcn_exp2f(x * __builtin_fmaf(x * x, c1, c0));
    return x * __builtin_amdgcn_rcpf(1.f + e);
}
#define STAMP_DECL
#define STAMP(i)
#define RSTAMP(i)
#define STAMP_FLUSH_K(k)
#define STAMP_ARG
#define STAMP_PASS
namespace k1 {
#define K1_SPARE_PF 0
#define K1_EPRIO 1
#define USE1(x)
constexpr int BM = 160, A_BYTES = BM * 128, B_BYTES = 64 * 256, NKS = 10;
constexpr int L_A = 0, L_B = 4 * A_BYTES, L_ST = L_B + 4 * B_BYTES, LDS_BYTES = L_ST + 4096;
enum { T_Q = 0, T_K = 1, T_V = 2, T_Z1 = 3, T_Z2 = 4 };
struct Params { const u16* x; const u16* wq; const u16* wk; const u16* wv; const u16* win; const u16* wsp; char* ws; };

struct Ld { rsrc_t ra, rb, rp; unsigned va[2], vb[2], sa, sb, sbinc, da, db, vp, sp, tp; const char* pk[4]; unsigned pinc[4]; int rot; };
#define K1_PFS 99
template <bool KV, int S> DI void ld_issue(Ld& L) {
    int gs = S + L.rot; if (gs >= NKS) gs -= NKS;
#pragma unroll
    for (int j = 0; j < 5; ++j) bdma(L.ra, L.va[j & 1], L.sa + gs * 128 + j * 8 * 1280, L.da + (S & 3) * A_BYTES + j * 1024);
#pragma unroll
    for (int j = 0; j < 4; ++j) {
        if constexpr (KV) glds(L.pk[j] + (unsigned)gs * L.pinc[j], L.db + (S & 3) * B_BYTES + j * 1024);
        else bdma(L.rb, L.vb[(j >> 1) & 1], L.sb + gs * L.sbinc + j * (L.sbinc >> 4), L.db + (S & 3) * B_BYTES + j * 1024);
    }
}
template <bool KV, int S> DI void ld_step(Ld& L) {
    if constexpr (S == 1) { ld_issue<KV, 2>(L); ld_issue<KV, 3>(L); }
    else if constexpr (S >= 2 && S + 2 < NKS) ld_issue<KV, S + 2>(L);
    if constexpr (S == K1_PFS) touch_line(L.tp, L.rp, L.vp, L.sp);
    constexpr int issued = S == 0 ? 1 : (S + 2 < NKS ? S + 2 : NKS - 1);
    asm volatile("s_waitcnt vmcnt(%0) lgkmcnt(0)\n\ts_barrier" :: "i"(9 * (issued - (S + 1)) + (S == K1_PFS ? 1 : 0)) : "memory");
    if constexpr (S == K1_PFS + 1) TOUCH_DONE(L.tp);
    if constexpr (S + 2 < NKS) ld_step<KV, S + 1>(L);
}
template <bool KV> DI void loader(Ld& L STAMP_ARG) {
    ld_issue<KV, 0>(L); ld_issue<KV, 1>(L);
    STAMP(2);
    WAIT_BAR(9);
    STAMP(3);
    ld_step<KV, 0>(L);
}

struct Frag { u32x4 x[5], w[2]; };
template <int S, int KS> DI void rd(Frag& f, lds_ptr lds, const unsigned (&xo)[5], unsigned wo0, unsigned wo1) {
    constexpr int sa = (S & 3) * A_BYTES, sb = (S & 3) * B_BYTES + KS * 8192;
#pragma unroll
    for (int n = 0; n < 2; ++n) {
        lds_ptr wp = lds + (n ? wo1 : wo0) + sb;
        const u32x2 lo = ldstr(wp), hi = ldstr(wp + 1024);
        f.w[n] = u32x4{lo[0], lo[1], hi[0], hi[1]};
    }
#pragma unroll
    for (int m = 0; m < 5; ++m) f.x[m] = ldsr128(lds + (xo[m] ^ (KS * 64)) + sa);
}
struct Stat { f32x4 s0, q0, s1, q1, cs; };
template <int S, int KS, int I> DI void rd1(Frag& f, lds_ptr lds, const unsigned (&xo)[5], unsigned wo0, unsigned wo1) {
    constexpr int sa = (S & 3) * A_BYTES, sb = (S & 3) * B_BYTES + KS * 8192;
    if constexpr (I < 4) {
        const u32x2 t = ldstr(lds + ((I >> 1) ? wo1 : wo0) + sb + (I & 1) * 1024);
        f.w[I >> 1][2 * (I & 1)] = t[0]; f.w[I >> 1][2 * (I & 1) + 1] = t[1];
    } else f.x[I - 4] = ldsr128(lds + (xo[I - 4] ^ (KS * 64)) + sa);
}
template <bool FT, int KSG, bool RD, int S, int KS>
DI void mm(const Frag& fc, Frag& fn, lds_ptr lds, const unsigned (&xo)[5], unsigned wo0, unsigned wo1, f32x4 (&acc)[5][2], int wc, Stat& st, const u32x4& ones) {
#define K1_MM(i) do { if (FT) mfma16_acc(acc[(i) >> 1][(i) & 1], fc.w[(i) & 1], fc.x[(i) >> 1]); else mfma16_acc(acc[(i) >> 1][(i) & 1], fc.x[(i) >> 1], fc.w[(i) & 1]); } while (0)
#define K1_RD(i) do { if constexpr (RD) rd1<S, KS, i>(fn, lds, xo, wo0, wo1); SBAR(); } while (0)
    K1_MM(0); K1_RD(0); K1_MM(1); K1_RD(1); K1_MM(2); K1_RD(2); K1_MM(3); K1_RD(3); K1_MM(4); K1_RD(4);
    K1_MM(5); K1_RD(5); K1_MM(6); K1_RD(6); K1_MM(7); K1_RD(7); K1_MM(8); K1_RD(8); K1_MM(9);
#undef K1_MM
#undef K1_RD
    mfma16_acc(st.s0, fc.x[0], ones); mfma16_acc(st.q0, fc.x[0], fc.x[0]); mfma16_acc(st.cs, ones, fc.w[0]);
    if ((KSG & 3) == wc) { mfma16_acc(st.s1, fc.x[4], ones); mfma16_acc(st.q1, fc.x[4], fc.x[4]); }
    SBAR();
}
template <bool FT, int S>
DI void stage(lds_ptr lds, Frag& f0, Frag& f1, const unsigned (&xo)[5], unsigned wo0, unsigned wo1, int wc,
              f32x4 (&acc)[5][2], Stat& st, const u32x4& ones STAMP_ARG) {
    if constexpr (S + 1 < NKS) BAR_LDS();
    STAMP(3 + S);
    mm<FT, 2 * S, true, S, 1>(f0, f1, lds, xo, wo0, wo1, acc, wc, st, ones);
    mm<FT, 2 * S + 1, (S + 1 < NKS), S + 1, 0>(f1, f0, lds, xo, wo0, wo1, acc, wc, st, ones);
    if constexpr (S + 1 < NKS) stage<FT, S + 1>(lds, f0, f1, xo, wo0, wo1, wc, acc, st, ones STAMP_PASS);
}
template <bool FT>
DI void mainloop(lds_ptr lds, int lane, int wr, int wc, f32x4 (&acc)[5][2], Stat& st STAMP_ARG) {
    const int r15 = lane & 15, g = lane >> 4;
    const unsigned xb = L_A + (80 * wr + r15) * 128 + ((g ^ ((r15 >> 1) & 7)) << 4);
    unsigned xo[5];
#pragma unroll
    for (int m = 0; m < 4; ++m) xo[m] = xb + (m ^ wc) * 2048;
    xo[4] = xb + 8192;
    const int q = r15 >> 2, p = lane & 3;
    const int fk = (q << 1) | ((g & 1) << 3);
    const unsigned wo0 = (L_B + (8 * g + q) * 256 + (((4 * wc + (p >> 1)) ^ fk) << 4) + 8 * (p & 1)) ^ (wr * 32), wo1 = wo0 ^ 32;
    Frag f0, f1;
    u32x4 ones = u32x4{0x3F803F80u, 0x3F803F80u, 0x3F803F80u, 0x3F803F80u};
    asm volatile("" : "+v"(ones), "+v"(st.s0), "+v"(st.q0), "+v"(st.s1), "+v"(st.q1), "+v"(st.cs));
#pragma unroll
    for (int m = 0; m < 5; ++m) asm volatile("" : "+v"(acc[m][0]), "+v"(acc[m][1]));
    BAR_LDS();
    rd<0, 0>(f0, lds, xo, wo0, wo1);
    stage<FT, 0>(lds, f0, f1, xo, wo0, wo1, wc, acc, st, ones STAMP_PASS);
    asm volatile("s_nop 7\n\ts_nop 7" ::: "memory");
}
template <int N> DI void gelu_n(f32x2* v) {
    f32x2 t[N];
#pragma unroll
    for (int i = 0; i < N; ++i) t[i] = v[i] * __builtin_elementwise_fma(v[i] * v[i], f32x2{-0.1029432397f, -0.1029432397f}, f32x2{-2.302208198f, -2.302208198f});
    SBAR();
#pragma unroll
    for (int i = 0; i < N; ++i) t[i] = f32x2{__builtin_amdgcn_exp2f(t[i][0]), __builtin_amdgcn_exp2f(t[i][1])};
    SBAR();
#pragma unroll
    for (int i = 0; i < N; ++i) t[i] += f32x2{1.f, 1.f};
#pragma unroll
    for (int i = 0; i < N; ++i) t[i] = f32x2{__builtin_amdgcn_rcpf(t[i][0]), __builtin_amdgcn_rcpf(t[i][1])};
    SBAR();
#pragma unroll
    for (int i = 0; i < N; ++i) v[i] *= t[i];
}
template <int TYPE>
DI void epilogue(char* ws, const f32x4 (&acc)[5][2], const LDSP float* csum, const LDSP float* ra, const LDSP float* rbv, int tw, int col0, int lane, int wr, int wc STAMP_ARG) {
    const int r15 = lane & 15, g = lane >> 4;
    constexpr bool FT = (TYPE == T_Q || TYPE == T_K || TYPE == T_Z1);
    float cf[2][4];
#pragma unroll
    for (int n = 0; n < 2; ++n) {
        if constexpr (FT) { const f32x4 c4 = *(const LDSP f32x4*)(csum + 32 * wc + 16 * (n ^ wr) + 4 * g); for (int j = 0; j < 4; ++j) cf[n][j] = c4[j]; }
        else cf[n][0] = csum[32 * wc + 16 * (n ^ wr) + r15];
    }
    f32x4 av[5], bv[5];
#pragma unroll
    for (int mp = 0; mp < 5; ++mp) {
        const int m = mp < 4 ? (mp ^ wc) : 4;
        if constexpr (FT) { av[mp][0] = ra[80 * wr + 16 * m + r15]; bv[mp][0] = rbv[80 * wr + 16 * m + r15]; }
        else { av[mp] = *(const LDSP f32x4*)(ra + 80 * wr + 16 * m + 4 * g); bv[mp] = *(const LDSP f32x4*)(rbv + 80 * wr + 16 * m + 4 * g); }
    }
    USE1(av[4][0]); USE1(bv[4][0]); STAMP(16);
    const rsrc_t rws = mk_rsrc(ws, (unsigned)OFF_Z2N);
#pragma unroll
    for (int mp = 0; mp < 5; ++mp) {
        const int m = mp < 4 ? (mp ^ wc) : 4;
        const int tb = tw + 16 * m;
        if (((wc >> 1) ^ mp) & 1) __builtin_amdgcn_s_setprio(1); else __builtin_amdgcn_s_setprio(0);
        f32x2 vv[4];
#pragma unroll
        for (int n = 0; n < 2; ++n)
#pragma unroll
            for (int j = 0; j < 2; ++j) {
                constexpr float sc = (TYPE == T_Q) ? 0.32259534218f : 1.f;
                const f32x2 ac = f32x2{acc[mp][n][2 * j], acc[mp][n][2 * j + 1]};
                if constexpr (FT) { const float a = av[mp][0] * sc, bb = bv[mp][0] * sc;
                                    vv[2 * n + j] = __builtin_elementwise_fma(f32x2{a, a}, ac, f32x2{bb, bb} * f32x2{cf[n][2 * j], cf[n][2 * j + 1]}); }
                else vv[2 * n + j] = __builtin_elementwise_fma(f32x2{av[mp][2 * j], av[mp][2 * j + 1]}, ac, f32x2{bv[mp][2 * j], bv[mp][2 * j + 1]} * f32x2{cf[n][0], cf[n][0]});
            }
        if constexpr (TYPE == T_Z1 || TYPE == T_Z2) { SBAR(); gelu_n<4>(vv); }
        const unsigned pa0 = pk2(vv[0][0], vv[0][1]), pa1 = pk2(vv[1][0], vv[1][1]), pb0 = pk2(vv[2][0], vv[2][1]), pb1 = pk2(vv[3][0], vv[3][1]);
        unsigned t00 = wr ? pb0 : pa0, t01 = wr ? pb1 : pa1, t10 = wr ? pa0 : pb0, t11 = wr ? pa1 : pb1;
        if constexpr (TYPE != T_K && TYPE != T_V) {
            auto x0 = __builtin_amdgcn_permlane16_swap(t00, t10, false, false);
            auto x1 = __builtin_amdgcn_permlane16_swap(t01, t11, false, false);
            t00 = x0[0]; t01 = x1[0]; t10 = x0[1]; t11 = x1[1];
        }
        asm volatile("" : "+v"(t00), "+v"(t01), "+v"(t10), "+v"(t11));
        if (tb < S_) {
            if constexpr (TYPE == T_K) {
                const int t = tb + r15;
                const unsigned kt = (unsigned)OFF_KF + (t >> 5) * KT_BYTES + (t & 31) * 16;
                st64(rws, kt + (g >> 1) * 512 + (g & 1) * 8, u32x2{t00, t01});
                if (g == 1) st64(rws, kt + 1024, u32x2{t10, t11});
                if (g == 2) st64(rws, kt + 1024 + 8, u32x2{0x00003F80u, 0u});
            } else if constexpr (TYPE == T_V) {
                const unsigned vt = (unsigned)OFF_VF + (tb >> 5) * VT_BYTES + ((((tb >> 4) & 1) * 2 + (g & 1)) * V_SLOTS) * 16 + (g >> 1) * 8;
                st64(rws, vt + r15 * 16, u32x2{t00, t01});
                if (r15 >= 4 && r15 < 8) st64(rws, vt + (12 + r15) * 16, u32x2{t10, t11});
                if (r15 == 8) st64(rws, vt + 20 * 16, u32x2{0x3F803F80u, 0x3F803F80u});
                if (r15 == 9) st64(rws, vt + 21 * 16, u32x2{0u, 0u});
            } else {
                unsigned dst;
                if constexpr (TYPE == T_Z2) dst = (unsigned)OFF_Z2R + (unsigned)(((tb >> 3) + (g >> 1)) * FH_ + col0 + 16 * (g & 1) + r15) * 16;
                else dst = (unsigned)(TYPE == T_Q ? OFF_Q : OFF_Z1) + (unsigned)((tb + r15) * (TYPE == T_Q ? 1280 : 1728) + (col0 + 16 * (g & 1) + 8 * (g >> 1)) * 2);
                st128(rws, dst, u32x4{t00, t01, t10, t11});
            }
        }
    }
}

__global__ __launch_bounds__(768, 3) void k1_proj(Params P) {
    extern __shared__ __attribute__((aligned(16))) char smem[];
    const int tid = threadIdx.x, lane = tid & 63, w = __builtin_amdgcn_readfirstlane(tid >> 6);
    STAMP_DECL; STAMP(0); RSTAMP(30);
    const int b = blockIdx.x, j0 = b >> 3, n0 = (b & 7) * 31 + j0;
    if (j0 == 31 || n0 >= 247) {
        if (b == 255 && tid < 64) ((u32x4*)(P.ws + OFF_FLAG))[tid * 8] = u32x4{0u, 0u, 0u, 0u};
        return;
    }
    int rb, cb;
    if (n0 < 133) { cb = n0 / 7; rb = n0 - cb * 7; } else { const int m = n0 - 133; cb = m / 6; rb = 7 + m - cb * 6; }
    const int row0 = rb * BM;
    lds_ptr lds = (lds_ptr)smem;
    const unsigned lds0 = (unsigned)(uintptr_t)smem;
    if (w >= 8) {
        const int L = w - 8;
        Ld ld;
        ld.ra = mk_rsrc(P.x, S_ * 1280);
        const int r8 = lane >> 3, c8 = lane & 7, k4 = lane >> 4, c16 = lane & 15;
#pragma unroll
        for (int i = 0; i < 2; ++i) ld.va[i] = r8 * 1280 + ((c8 ^ ((4 * ((L + i) & 1) + k4) & 7)) << 4);
        ld.sa = (row0 + 40 * L) * 1280;
        ld.da = lds0 + L_A + 5 * L * 1024; ld.db = lds0 + L_B + 4 * L * 1024;
        const unsigned ldw = cb < 5 ? 1280u : 3456u;
        ld.rb = cb < 5 ? mk_rsrc(P.wq, D_ * 1280) : mk_rsrc(P.win, D_ * 3456);
#pragma unroll
        for (int i = 0; i < 2; ++i) ld.vb[i] = k4 * ldw + ((c16 ^ (k4 << 1) ^ (i << 3)) << 4);
        ld.sb = (cb < 5 ? cb : cb - 5) * 256 + 16 * L * ldw; ld.sbinc = 64 * ldw;
        ld.rot = 0;
        ld.rp = mk_rsrc(P.wsp, S_ * 4096); ld.vp = (lane >> 5) * 4096 + (lane & 31) * 128; ld.sp = (unsigned)(b * 8 + L * 2) * 4096; ld.tp = 0;
        STAMP(1);
        if (cb != 18) loader<false>(ld STAMP_PASS);
        else {
#pragma unroll
            for (int j = 0; j < 4; ++j) {
                const int k = 16 * L + 4 * j + k4, c = c16 ^ (k4 << 1) ^ (((j >> 1) & 1) << 3), cc = c & 3;
                if (c < 8) { ld.pk[j] = (const char*)P.win + k * 3456 + 13 * 256 + c * 16; ld.pinc[j] = 64 * 3456; }
                else { ld.pk[j] = (const char*)(c < 12 ? P.wk : P.wv) + k * 40 + (cc == 1 ? 16 : cc == 2 ? 24 : 0); ld.pinc[j] = 64 * 40; }
            }
            loader<true>(ld STAMP_PASS);
        }
        return;
    }
    unsigned tch = 0;
    {
        const int wg = n0;
        if (w == 0) touch_line(tch, mk_rsrc(P.x, S_ * 1280), (unsigned)(wg * 83 + lane) * 128, 0);
        else if (w == 1) touch_line(tch, mk_rsrc(P.x, S_ * 1280), (unsigned)(wg * 83 + min(64 + lane, 82)) * 128, 0);
        else if (w == 2) touch_line(tch, mk_rsrc(P.win, D_ * 3456), (unsigned)(wg * 70 + lane) * 128, 0);
        else if (w == 3) touch_line(tch, mk_rsrc(P.win, D_ * 3456), (unsigned)(wg * 70 + min(64 + lane, 69)) * 128, 0);
        else if (w == 4) touch_line(tch, mk_rsrc(P.wq, D_ * 1280), (unsigned)(wg * 26 + min(lane, 25)) * 128, 0);
    }
    const int wr = w & 1, wc = w >> 1;
    int type, col0;
    if (cb < 5) { type = T_Q; col0 = cb * 128 + wc * 32; }
    else { const int zc = (cb - 5) * 128 + wc * 32;
           if (cb == 18 && wc >= 2) { type = (wc == 2) ? T_K : T_V; col0 = 0; }
           else if (zc < FH_) { type = T_Z1; col0 = zc; } else { type = T_Z2; col0 = zc - FH_; } }
    const bool ft = (type == T_Q || type == T_K || type == T_Z1);
    f32x4 acc[5][2];
#pragma unroll
    for (int m = 0; m < 5; ++m) { acc[m][0] = f32x4{0, 0, 0, 0}; acc[m][1] = f32x4{0, 0, 0, 0}; }
    Stat st; st.s0 = st.q0 = st.s1 = st.q1 = st.cs = f32x4{0, 0, 0, 0};
    STAMP(2);
    if (ft) mainloop<true>(lds, lane, wr, wc, acc, st STAMP_PASS);
    else mainloop<false>(lds, lane, wr, wc, acc, st STAMP_PASS);
    WAIT_VM(0); TOUCH_DONE(tch);
    STAMP(13);
    const int r15 = lane & 15, g = lane >> 4;
    LDSP float* rsum = (LDSP float*)(lds + L_ST); LDSP float* rsq = rsum + 160; LDSP float* csum = rsum + 320; LDSP float* ra = rsum + 448; LDSP float* rbv = rsum + 608;
    LDSP float* p4s = rsum + 768; LDSP float* p4q = rsum + 896;
    if (r15 == 0) { *(LDSP f32x4*)(rsum + 80 * wr + 16 * wc + 4 * g) = st.s0; *(LDSP f32x4*)(p4s + wc * 32 + wr * 16 + 4 * g) = st.s1; }
    if ((r15 >> 2) == g) {
        const int j = r15 & 3;
        rsq[80 * wr + 16 * wc + r15] = j == 0 ? st.q0[0] : j == 1 ? st.q0[1] : j == 2 ? st.q0[2] : st.q0[3];
        p4q[wc * 32 + wr * 16 + r15] = j == 0 ? st.q1[0] : j == 1 ? st.q1[1] : j == 2 ? st.q1[2] : st.q1[3];
    }
    if (g == 0) csum[32 * wc + 16 * wr + r15] = st.cs[0];
    STAMP(19);
    BAR_LDS();
    STAMP(20);
    if (tid < BM) {
        const int tr = tid >= 80, tl = tid - 80 * tr, o = tr * 16 + (tl & 15);
        float sm = rsum[tid], sq = rsq[tid];
        const float s4 = p4s[o] + p4s[32 + o] + p4s[64 + o] + p4s[96 + o], q4 = p4q[o] + p4q[32 + o] + p4q[64 + o] + p4q[96 + o];
        if (tl >= 64) { sm = s4; sq = q4; }
        const float mu = sm * (1.f / D_), var = fmaxf(sq * (1.f / D_) - mu * mu, 0.f);
        const float rs = __builtin_amdgcn_rsqf(var + 1e-5f);
        ra[tid] = rs; rbv[tid] = -mu * rs;
    }
    BAR_LDS();
    STAMP(14);
    const int tw = row0 + 80 * wr;
    switch (type) {
        case T_Q: epilogue<T_Q>(P.ws, acc, csum, ra, rbv, tw, col0, lane, wr, wc STAMP_PASS); break;
        case T_K: epilogue<T_K>(P.ws, acc, csum, ra, rbv, tw, col0, lane, wr, wc STAMP_PASS); break;
        case T_V: epilogue<T_V>(P.ws, acc, csum, ra, rbv, tw, col0, lane, wr, wc STAMP_PASS); break;
        case T_Z1: epilogue<T_Z1>(P.ws, acc, csum, ra, rbv, tw, col0, lane, wr, wc STAMP_PASS); break;
        default: epilogue<T_Z2>(P.ws, acc, csum, ra, rbv, tw, col0, lane, wr, wc STAMP_PASS); break;
    }
    STAMP(15); RSTAMP(31); STAMP_FLUSH_K(0);
}
}
namespace k2 {
constexpr int KV_HALF = 12288, KV_STAGE = 2 * KV_HALF, W_STAGE = 32768;
constexpr int L_KV = 0, L_W = 3 * KV_STAGE, L_RED = L_W + 2 * W_STAGE, L_DUMMY = L_RED + 1024, L_OUT = L_DUMMY + 6144, LDS_BYTES = L_OUT + 8 * 1280;
enum { C_KV0 = 64, C_KV12, C_GAT, C_NORM, C_PUB, C_FLAG, C_ATT = 72  , C_PAIR = 80, C_PROG = 88  , C_UST = 70 };
DI void lds_add(lds_ptr lds, int word) {
    asm volatile("s_mov_b64 exec, 1\n\tds_add_u32 %0, %1\n\ts_mov_b64 exec, -1" :: "v"((unsigned)(L_RED + 4 * word) + (unsigned)(uintptr_t)lds), "v"(1u) : "memory");
}
DI void lds_wait_ge(lds_ptr lds, int word, unsigned target) {
    const volatile LDSP unsigned* p = (const volatile LDSP unsigned*)(lds + L_RED + 4 * word);
    for (unsigned spin = 0; spin < (1u << 26); ++spin) {
        if ((unsigned)__builtin_amdgcn_readfirstlane(*p) >= target) break;
        __builtin_amdgcn_s_sleep(1);
    }
    asm volatile("" ::: "memory");
}
constexpr float M_SHIFT = 100.f;
constexpr int ZSTEP = 2 * FH_ * 16;
struct Params { const u16* wsp; const u16* bsp; const u16* wo; const u16* wout; const u16* x; char* ws; };
#define USE(x) asm volatile("" :: "v"(x))
#define EXP2(x) __builtin_amdgcn_exp2f(x)
DI float lanemax16(const f32x16& s) {
    float a = fmaxf(fmaxf(s[0], s[1]), s[2]), b = fmaxf(fmaxf(s[3], s[4]), s[5]);
#pragma unroll
    for (int r = 6; r < 14; r += 4) { a = fmaxf(fmaxf(a, s[r]), s[r + 1]); b = fmaxf(fmaxf(b, s[r + 2]), s[r + 3]); }
    a = fmaxf(fmaxf(a, s[14]), s[15]);
    return fmaxf(a, b);
}
DI float halves_max(float a) {
    auto rr = __builtin_amdgcn_permlane32_swap(__float_as_uint(a), __float_as_uint(a), false, false);
    return fmaxf(__uint_as_float(rr[0]), __uint_as_float(rr[1]));
}
DI float rowmax32(const f32x16& s) { return halves_max(lanemax16(s)); }
DI void pack16(const f32x16& s, u32x4& p0, u32x4& p1) {
    p0 = u32x4{pk2(s[0], s[1]), pk2(s[2], s[3]), pk2(s[4], s[5]), pk2(s[6], s[7])};
    p1 = u32x4{pk2(s[8], s[9]), pk2(s[10], s[11]), pk2(s[12], s[13]), pk2(s[14], s[15])};
}
DI f32x16 attn_fallback(const char* ws, u32x4 q0, u32x2 q1, int lane) {
    const int r = lane & 31, h = lane >> 5;
    if (h) q1 = u32x2{0u, 0u};
    f32x16 o = {}; float m = -INFINITY;
#pragma unroll 1
    for (int T = 0; T < 64; ++T) {
        const char* kt = ws + OFF_KF + T * KT_BYTES; const char* vt = ws + OFF_VF + T * VT_BYTES;
        const u32x4 k0 = *(const u32x4*)(kt + h * 512 + r * 16); const u32x2 k1 = *(const u32x2*)(kt + 1024 + r * 16 + h * 8);
        const int rv = min(r, 21);
        const u32x4 v0 = *(const u32x4*)(vt + (h * V_SLOTS + rv) * 16), v1 = *(const u32x4*)(vt + ((2 + h) * V_SLOTS + rv) * 16);
        f32x16 s = mfma32(k0, q0, f32x16{}); s = mfma32k8(k1, q1, s);
        const float mn = fmaxf(m, rowmax32(s));
        const float al = (mn == -INFINITY) ? 1.f : EXP2(m - mn);
        const float ms = (mn == -INFINITY) ? 0.f : mn;
#pragma unroll
        for (int i = 0; i < 16; ++i) { s[i] = EXP2(s[i] - ms); o[i] *= al; }
        m = mn;
        u32x4 p0, p1; pack16(s, p0, p1);
        o = mfma32(v0, p0, o); o = mfma32(v1, p1, o);
    }
    return o;
}

struct St {
    u32x4 qa0, qb0; u32x2 qa1, qb1;
    f32x16 sa, sb, oa, ob;
    u32x4 pa0, pa1, pb0, pb1;
    u32x4 k0[2]; u32x2 k1[2]; u32x4 v0[2], v1[2];
};
#define K2_SCHED 4
#define E_(S, i) S[i] = EXP2(S[i]); USE(S[i])
#define C_(P, j, S, i) P[j] = pk2(S[i], S[i + 1]); USE(P[j])
#define K2_STEP(S, P0, P1, SP, PP1, M0, M1, M2, M3, R0, R1, R2)                                          \
    do {                                                                                                 \
        C_(PP1, 3, SP, 14); M0; R0; SBAR(); E_(S, 0); E_(S, 1); E_(S, 2); E_(S, 3); SBAR();              \
        M1; R1; C_(P0, 0, S, 0); E_(S, 4); E_(S, 5); C_(P0, 1, S, 2); E_(S, 6); E_(S, 7); SBAR();        \
        M2; R2; C_(P0, 2, S, 4); E_(S, 8); E_(S, 9); C_(P0, 3, S, 6); E_(S, 10); E_(S, 11); SBAR();      \
        M3; C_(P1, 0, S, 8); E_(S, 12); E_(S, 13); C_(P1, 1, S, 10); E_(S, 14); E_(S, 15); C_(P1, 2, S, 12); SBAR(); \
    } while (0)
#define K2_FLIP 1
#define K2_PLO 1
#define K2_PHI 2
#define K2_ALIGN 1
template <int STEP> DI void prio_step() {
    if constexpr (STEP == 0) __builtin_amdgcn_s_setprio(3);
    if constexpr (STEP == 3) __builtin_amdgcn_s_setprio(2);
    if constexpr (STEP == 6) __builtin_amdgcn_s_setprio(1);
}
struct Pr { volatile LDSP unsigned* mine; const volatile LDSP unsigned* other; unsigned it, seen; };
DI void prio_rel(Pr& p) {
}
template <int I>
DI void att_iter(St& s, Pr& pr, lds_ptr pk0, lds_ptr pk1, lds_ptr pv) {
    constexpr int c = I & 1, n = c ^ 1;
    prio_rel(pr);
    prio_step<2 * I>();
    K2_STEP(s.sa, s.pa0, s.pa1, s.sb, s.pb1,
            s.sb = mfma32(s.k0[c], s.qb0, f32x16{}),
            s.sb = mfma32k8(s.k1[c], s.qb1, s.sb),
            s.ob = mfma32(s.v0[n], s.pb0, s.ob),
            s.ob = mfma32(s.v1[n], s.pb1, s.ob),
            { s.k0[n] = ldsr128(pk0 + I * KT_BYTES); s.k1[n] = *(const LDSP u32x2*)(pk1 + I * KT_BYTES); },
            { s.v0[c] = ldsr128(pv + I * VT_BYTES); },
            { s.v1[c] = ldsr128(pv + I * VT_BYTES + 2 * V_SLOTS * 16); });
    prio_step<2 * I + 1>();
    K2_STEP(s.sb, s.pb0, s.pb1, s.sa, s.pa1,
            s.sa = mfma32(s.k0[n], s.qa0, f32x16{}),
            s.sa = mfma32k8(s.k1[n], s.qa1, s.sa),
            s.oa = mfma32(s.v0[c], s.pa0, s.oa),
            s.oa = mfma32(s.v1[c], s.pa1, s.oa),
            {}, {}, {});
}
DI void att_loop(St& s, lds_ptr lds, lds_ptr kvb, unsigned k0o, unsigned k1o, unsigned vo, int w STAMP_ARG) {
    int buf = 0;
    Pr pr; pr.mine = (volatile LDSP unsigned*)(lds + L_RED) + C_PROG + w; pr.other = (const volatile LDSP unsigned*)(lds + L_RED) + C_PROG + (w ^ 4); pr.it = 0; pr.seen = 0;
    const volatile LDSP unsigned* cw = (const volatile LDSP unsigned*)(lds + L_RED);
    unsigned seen = 0;
#pragma unroll 1
    for (int ss = 0; ss < 8; ++ss) {
        const int word = ss == 0 ? C_KV0 : ss == 1 ? C_KV12 : C_GAT;
        const unsigned target = ss == 1 ? 8u : ss < 3 ? 0u : 4u * (ss - 1);
        if ((unsigned)__builtin_amdgcn_readfirstlane(seen) < target) lds_wait_ge(lds, word, target);
        if (ss > 0) lds_wait_ge(lds, C_ATT + ss - 1, 8u);
        lds_ptr kv = kvb + buf * KV_STAGE;
        att_iter<0>(s, pr, kv + k0o, kv + k1o, kv + vo);
        if (ss == 0) { WAIT_VM(0); lds_add(lds, C_KV12); }
        att_iter<1>(s, pr, kv + k0o, kv + k1o, kv + vo);
        att_iter<2>(s, pr, kv + k0o, kv + k1o, kv + vo);
        seen = cw[ss == 0 ? C_KV12 : C_GAT];
        att_iter<3>(s, pr, kv + k0o, kv + k1o, kv + vo);
        asm volatile("s_waitcnt lgkmcnt(0)" ::: "memory");
        lds_add(lds, C_ATT + ss);
        buf = (buf == 2) ? 0 : buf + 1;
    }
}
DI void attention(const Params& P, lds_ptr lds, unsigned lds0, int b, int w, int lane STAMP_ARG) {
    const int p = w & 3, hf = w >> 2, r31 = lane & 31, h = lane >> 5;
    char* ws = P.ws;
    St s;
    __builtin_amdgcn_s_setprio(2);
    BAR_LDS();
    const rsrc_t rkv = mk_rsrc(ws, (unsigned)OFF_Z1);
    unsigned skv[3], kinc[3];
#pragma unroll
    for (int i = 0; i < 3; ++i) {
        const int pc = 3 * w + i, ph = pc >= 12, j = pc - ph * 12;
        skv[i] = j < 6 ? (unsigned)OFF_KF + (ph * 32 + 1) * KT_BYTES + j * 1024 : (unsigned)OFF_VF + (ph * 32) * VT_BYTES + (j - 6) * 1024;
        kinc[i] = j < 6 ? 4 * KT_BYTES : 4 * VT_BYTES;
    }
    const unsigned kvdst = lds0 + L_KV + 3 * w * 1024;
    STAMP(18);
#pragma unroll
    for (int i = 0; i < 3; ++i) bdma(rkv, lane * 16, skv[i], kvdst + i * 1024);
#pragma unroll
    for (int i = 0; i < 2; ++i) {
        const int pc = min(2 * w + i, 13);
        const unsigned src = pc < 10 ? (unsigned)OFF_Q + (8 * b) * 1280 + pc * 1024 : (unsigned)OFF_KF + (pc >= 12 ? 32 * KT_BYTES : 0) + (pc & 1) * 1024;
        bdma(rkv, lane * 16, src, lds0 + L_W + W_STAGE + pc * 1024);
    }
    STAMP(19);
    WAIT_VM(0);
    lds_add(lds, C_KV0);
#pragma unroll
    for (int st = 1; st < 3; ++st)
#pragma unroll
        for (int i = 0; i < 3; ++i) bdma(rkv, lane * 16, skv[i] + st * kinc[i], kvdst + st * KV_STAGE + i * 1024);
    STAMP(20);
    lds_wait_ge(lds, C_KV0, 8u);
    STAMP(21);
    const int tA = 8 * b + 2 * p;
    {
        lds_ptr qp = lds + L_W + W_STAGE + (2 * p) * 1280 + r31 * 40;
        auto rd16 = [&](lds_ptr a) { const u32x2 lo = *(const LDSP u32x2*)a, hi = *(const LDSP u32x2*)(a + 8); return u32x4{lo[0], lo[1], hi[0], hi[1]}; };
        s.qa0 = rd16(qp + h * 16); s.qb0 = rd16(qp + 1280 + h * 16);
        s.qa1 = *(const LDSP u32x2*)(qp + 32); s.qb1 = *(const LDSP u32x2*)(qp + 1280 + 32);
        lds_ptr k0p = lds + L_W + W_STAGE + 10240;
        const u32x4 k00 = ldsr128(k0p + h * 512 + r31 * 16); const u32x2 k01 = *(const LDSP u32x2*)(k0p + 1024 + r31 * 16 + h * 8);
        lds_ptr kfp = k0p + hf * 2048;
        s.k0[0] = ldsr128(kfp + h * 512 + r31 * 16); s.k1[0] = *(const LDSP u32x2*)(kfp + 1024 + r31 * 16 + h * 8);
        if (h) { s.qa1 = u32x2{0u, 0u}; s.qb1 = u32x2{0u, 0u}; }
        f32x16 a = mfma32(k00, s.qa0, f32x16{}), c = mfma32(k00, s.qb0, f32x16{});
        a = mfma32k8(k01, s.qa1, a); c = mfma32k8(k01, s.qb1, c);
        float ma = lanemax16(a), mb = lanemax16(c);
#pragma unroll
        for (int i = 0; i < 3; ++i) {
            lds_ptr kp = lds + L_KV + i * KT_BYTES;
            const u32x4 kx0 = ldsr128(kp + h * 512 + r31 * 16); const u32x2 kx1 = *(const LDSP u32x2*)(kp + 1024 + r31 * 16 + h * 8);
            a = mfma32(kx0, s.qa0, f32x16{}); c = mfma32(kx0, s.qb0, f32x16{});
            a = mfma32k8(kx1, s.qa1, a); c = mfma32k8(kx1, s.qb1, c);
            ma = fmaxf(ma, lanemax16(a)); mb = fmaxf(mb, lanemax16(c));
        }
        ma = halves_max(ma) + M_SHIFT; mb = halves_max(mb) + M_SHIFT;
        if (h) { s.qa1[0] = pk2(-ma, 0.f); s.qb1[0] = pk2(-mb, 0.f); }
    }
    s.sa = mfma32(s.k0[0], s.qa0, f32x16{}); s.sa = mfma32k8(s.k1[0], s.qa1, s.sa);
    s.oa = f32x16{}; s.ob = f32x16{}; s.sb = f32x16{};
    s.pb0 = u32x4{0, 0, 0, 0}; s.pb1 = u32x4{0, 0, 0, 0}; s.v0[1] = u32x4{0, 0, 0, 0}; s.v1[1] = u32x4{0, 0, 0, 0};
    lds_ptr kvb = lds + L_KV + hf * KV_HALF;
    const unsigned k0o = h * 512 + r31 * 16, k1o = 1024 + r31 * 16 + h * 8, vo = 6144 + (h * V_SLOTS + min(r31, 21)) * 16;
    STAMP(3);
    att_loop(s, lds, kvb, k0o, k1o, vo, w STAMP_PASS);
    STAMP(15);
    s.pb1[3] = pk2(s.sb[14], s.sb[15]);
    s.ob = mfma32(s.v0[1], s.pb0, s.ob); s.ob = mfma32(s.v1[1], s.pb1, s.ob);
    __builtin_amdgcn_s_setprio(3);
    lds_wait_ge(lds, C_ATT + 5, 8u);
    LDSP f32x4* xs = (LDSP f32x4*)(lds + L_KV + 2 * KV_STAGE) + w * 3 * 64;
    LDSP f32x4* xr = (LDSP f32x4*)(lds + L_KV + 2 * KV_STAGE) + (w ^ 4) * 3 * 64;
    if (hf == 0) {
#pragma unroll
        for (int i = 0; i < 3; ++i) xs[i * 64 + lane] = f32x4{s.ob[4 * i], s.ob[4 * i + 1], s.ob[4 * i + 2], s.ob[4 * i + 3]};
    } else {
#pragma unroll
        for (int i = 0; i < 3; ++i) xs[i * 64 + lane] = f32x4{s.oa[4 * i], s.oa[4 * i + 1], s.oa[4 * i + 2], s.oa[4 * i + 3]};
        s.oa = s.ob; s.qa0 = s.qb0; s.qa1 = s.qb1;
    }
    asm volatile("s_waitcnt lgkmcnt(0)" ::: "memory");
    lds_add(lds, C_PAIR + w);
    lds_wait_ge(lds, C_PAIR + (w ^ 4), 1u);
    STAMP(16);
    f32x16 o = s.oa;
#pragma unroll
    for (int i = 0; i < 3; ++i) { const f32x4 t = xr[i * 64 + lane]; o[4 * i] += t[0]; o[4 * i + 1] += t[1]; o[4 * i + 2] += t[2]; o[4 * i + 3] += t[3]; }
    auto lr = __builtin_amdgcn_permlane32_swap(__float_as_uint(o[8]), __float_as_uint(o[8]), false, false);
    float l = __uint_as_float(lr[1]);
    bool ok = (l >= 0x1p-110f) && (l <= 0x1p120f);
#pragma unroll
    for (int i = 0; i < 12; ++i) ok = ok && (fabsf(o[i]) <= 0x1p126f || (h && i >= 8));
    if (__builtin_expect(!__all(ok), 0)) {
        o = attn_fallback(ws, s.qa0, s.qa1, lane);
        lr = __builtin_amdgcn_permlane32_swap(__float_as_uint(o[8]), __float_as_uint(o[8]), false, false);
        l = __uint_as_float(lr[1]);
    }
    const float rl = 1.f / l;
    lds_ptr ob = lds + L_OUT + w * 1280;
    *(LDSP u32x2*)(ob + r31 * 40 + h * 8) = u32x2{pk2(o[0] * rl, o[1] * rl), pk2(o[2] * rl, o[3] * rl)};
    *(LDSP u32x2*)(ob + r31 * 40 + h * 8 + 16) = u32x2{pk2(o[4] * rl, o[5] * rl), pk2(o[6] * rl, o[7] * rl)};
    if (!h) *(LDSP u32x2*)(ob + r31 * 40 + 32) = u32x2{pk2(o[8] * rl, o[9] * rl), pk2(o[10] * rl, o[11] * rl)};
    asm volatile("s_waitcnt lgkmcnt(0)" ::: "memory");
    const rsrc_t rou = mk_rsrc(ws + OFF_OU, S_ * 3072);
    st128(rou, (unsigned)((tA + hf) * 3072 + lane * 16), ldsr128(ob + lane * 16));
    if (lane < 16) st128(rou, (unsigned)((tA + hf) * 3072 + 1024 + lane * 16), ldsr128(ob + 1024 + lane * 16));
}

DI void zload(u32x4& z, rsrc_t r, unsigned voff, unsigned soff) {
    asm volatile("buffer_load_dwordx4 %0, %1, %2, %3 offen sc1" : "+v"(z) : "v"(voff), "s"(r), "s"(soff) : "memory");
}
#define K2_GSPLIT 1
#define K2_PFSS 4
#define K2_POLL 16
#define GBURST_PRIO 2
#define GSTEP_PRIO 3
struct Gs {
    rsrc_t rkv, rw, rz; unsigned vkv, vw[8], skv[6], sw, wstr, dkv, dw, zoff;
    u32x4 z[16]; f32x16 g0, g1; u32x4 wa0, wa1;
};
template <int I> DI void kv_dma(const Gs& g) { bdma(g.rkv, g.vkv, g.skv[I], g.dkv + I * 1024); }
template <int I> DI void w_dma(const Gs& g) { bdma(g.rw, g.vw[I], g.sw + I * g.wstr, g.dw + I * 1024); }
DI void z_loads(Gs& g, unsigned zs) {
#pragma unroll
    for (int i = 0; i < 16; ++i) zload(g.z[i], g.rz, g.zoff, zs + i * ZSTEP);
}
template <int I> DI void kv_all(const Gs& g) { kv_dma<I>(g); if constexpr (I < 5) kv_all<I + 1>(g); }
template <int I> DI void w_all(const Gs& g) { w_dma<I>(g); if constexpr (I < 7) w_all<I + 1>(g); }
#define Z_TIE(g) "+v"(g.z[0]), "+v"(g.z[1]), "+v"(g.z[2]), "+v"(g.z[3]), "+v"(g.z[4]), "+v"(g.z[5]), "+v"(g.z[6]), "+v"(g.z[7]), \
                 "+v"(g.z[8]), "+v"(g.z[9]), "+v"(g.z[10]), "+v"(g.z[11]), "+v"(g.z[12]), "+v"(g.z[13]), "+v"(g.z[14]), "+v"(g.z[15])
template <int I> DI void g_burst(Gs& g, lds_ptr wp, unsigned wx) {
    u32x4 wn0, wn1;
    if constexpr (I == 0) { g.wa0 = ldsr128(wp + wx); g.wa1 = ldsr128(wp + wx + 16384); }
    if constexpr (I < 15) { wn0 = ldsr128(wp + (wx ^ ((I + 1) * 32))); wn1 = ldsr128(wp + (wx ^ ((I + 1) * 32)) + 16384); }
    g.g0 = mfma32(g.z[I], g.wa0, g.g0); g.g1 = mfma32(g.z[I], g.wa1, g.g1);
    if constexpr (I < 15) { g.wa0 = wn0; g.wa1 = wn1; }
    SBAR();
    if constexpr (I < 15) g_burst<I + 1>(g, wp, wx);
}
template <int J> DI void dma_piece(const Gs& g) { if constexpr (J < 8) w_dma<J>(g); else kv_dma<J - 8>(g); }
template <int N> DI void z_wait(u32x4& z) { asm volatile("s_waitcnt vmcnt(%1)" : "+v"(z) : "i"(N) : "memory"); }
template <int I, bool FIRST> DI void g_step(Gs& g, lds_ptr wp, unsigned wx, unsigned zs) {
    u32x4 wn0, wn1;
    if constexpr (I == 0) { g.wa0 = ldsr128(wp + wx); g.wa1 = ldsr128(wp + wx + 16384); }
    if constexpr (I < 15) { wn0 = ldsr128(wp + (wx ^ ((I + 1) * 32))); wn1 = ldsr128(wp + (wx ^ ((I + 1) * 32)) + 16384); }
    constexpr int nd = I < 3 ? 2 : I < 11 ? 1 : 0;
    constexpr int before = I <= 3 ? 2 * I : I <= 11 ? I + 3 : 14;
    z_wait<FIRST ? 15 + before : 29 - nd>(g.z[I]);
    g.g0 = mfma32(g.z[I], g.wa0, g.g0);
    SBAR();
    if constexpr (I < 3) { dma_piece<2 * I>(g); dma_piece<2 * I + 1>(g); } else if constexpr (I < 11) dma_piece<I + 3>(g);
    else __builtin_amdgcn_s_sleep(1);
    SBAR();
    g.g1 = mfma32(g.z[I], g.wa1, g.g1);
    zload(g.z[I], g.rz, g.zoff, zs + I * ZSTEP);
    if constexpr (I < 15) { g.wa0 = wn0; g.wa1 = wn1; }
    SBAR();
    if constexpr (I < 15) g_step<I + 1, FIRST>(g, wp, wx, zs);
}
template <bool HASG>
DI void gating(const Params& P, lds_ptr lds, unsigned lds0, int b, int gw, int lane, int t0, int f0 STAMP_ARG) {
    char* ws = P.ws;
    const int t256 = gw * 64 + lane, r31 = lane & 31, h = lane >> 5;
    __builtin_amdgcn_s_setprio(3);
    const char* zr0 = ws + OFF_Z2R + (unsigned)(b * FH_ * 16);
    u32x4 c[4];
#pragma unroll
    for (int i = 0; i < 4; ++i) c[i] = *(const u32x4*)(zr0 + (i < 3 || t256 < FH_ - 768 ? t256 + 256 * i : t256) * 16);
    if (gw == 0 && lane < 32) ((LDSP unsigned*)(lds + L_RED))[64 + lane] = 0u;
    BAR_LDS();
    STAMP(21);
    USE(c[0][0]); USE(c[1][0]); USE(c[2][0]); USE(c[3][0]);
    STAMP(22);
    if (t256 >= FH_ - 768) c[3] = u32x4{0, 0, 0, 0};
    LDSP float* red = (LDSP float*)(lds + L_RED);
    {
        float v[16];
#pragma unroll
        for (int j = 0; j < 8; ++j) { v[j] = 0.f; v[8 + j] = 0.f; }
#pragma unroll
        for (int i = 0; i < 4; ++i)
#pragma unroll
            for (int j = 0; j < 4; ++j) {
                const float lo = bflo(c[i][j]), hi = bfhi(c[i][j]);
                v[2 * j] += lo; v[2 * j + 1] += hi; v[8 + 2 * j] = __builtin_fmaf(lo, lo, v[8 + 2 * j]); v[8 + 2 * j + 1] = __builtin_fmaf(hi, hi, v[8 + 2 * j + 1]);
            }
        float u[8], t[4];
#pragma unroll
        for (int j = 0; j < 8; ++j) { auto r = __builtin_amdgcn_permlane32_swap(__float_as_uint(v[j]), __float_as_uint(v[j + 8]), false, false); u[j] = __uint_as_float(r[0]) + __uint_as_float(r[1]); }
#pragma unroll
        for (int j = 0; j < 4; ++j) { auto r = __builtin_amdgcn_permlane16_swap(__float_as_uint(u[j]), __float_as_uint(u[j + 4]), false, false); t[j] = __uint_as_float(r[0]) + __uint_as_float(r[1]); }
#pragma unroll
        for (int j = 0; j < 4; ++j) {
            t[j] += __uint_as_float(__builtin_amdgcn_update_dpp(0, __float_as_uint(t[j]), 0x128, 0xf, 0xf, false));
            t[j] += __uint_as_float(__builtin_amdgcn_update_dpp(0, __float_as_uint(t[j]), 0x124, 0xf, 0xf, false));
            t[j] += __uint_as_float(__builtin_amdgcn_update_dpp(0, __float_as_uint(t[j]), 0x122, 0xf, 0xf, false));
            t[j] += __uint_as_float(__builtin_amdgcn_update_dpp(0, __float_as_uint(t[j]), 0x121, 0xf, 0xf, false));
        }
        if ((lane & 15) == 0) { const int rho = lane >> 4; *(LDSP f32x4*)(red + gw * 16 + (rho >> 1) * 8 + (rho & 1) * 4) = f32x4{t[0], t[1], t[2], t[3]}; }
        asm volatile("s_waitcnt lgkmcnt(0)" ::: "memory");
        lds_add(lds, C_NORM);
        STAMP(2);
        lds_wait_ge(lds, C_NORM, 4u);
        float sm = 0, sq = 0;
#pragma unroll
        for (int j = 0; j < 4; ++j) { sm += red[j * 16 + (lane & 7)]; sq += red[j * 16 + 8 + (lane & 7)]; }
        const float mu = sm * (1.f / FH_), var = fmaxf(sq * (1.f / FH_) - mu * mu, 0.f), rs = __builtin_amdgcn_rsqf(var + 1e-5f), sh = -mu * rs;
        float a[8], bb[8];
#pragma unroll
        for (int j = 0; j < 8; ++j) { a[j] = __uint_as_float(__builtin_amdgcn_readlane(__float_as_uint(rs), j)); bb[j] = __uint_as_float(__builtin_amdgcn_readlane(__float_as_uint(sh), j)); }
        const rsrc_t zw = mk_rsrc(ws + OFF_Z2N + (unsigned)(b * FH_ * 16), FH_ * 16);
#pragma unroll
        for (int i = 0; i < 4; ++i) {
            u32x4 o;
#pragma unroll
            for (int j = 0; j < 4; ++j) o[j] = pk2(__builtin_fmaf(bflo(c[i][j]), a[2 * j], bb[2 * j]), __builtin_fmaf(bfhi(c[i][j]), a[2 * j + 1], bb[2 * j + 1]));
            __builtin_amdgcn_raw_buffer_store_b128(o, zw, (t256 + 256 * i) * 16, 0, 16);
        }
    }
    STAMP(23);
    Gs g;
    g.rkv = mk_rsrc(ws + OFF_KF, (unsigned)(OFF_Z1 - OFF_KF)); g.rw = mk_rsrc(P.wsp, S_ * 4096); g.rz = mk_rsrc(ws + OFF_Z2N, (unsigned)(S_ * FH_ * 2 + 4096));
    g.vkv = lane * 16;
#pragma unroll
    for (int i = 0; i < 8; ++i) g.vw[i] = (lane >> 5) * 4096 + (((lane & 31) ^ ((2 * i + (lane >> 5)) & 15)) << 4);
    const int rot = 0;
    const unsigned sw0 = (t0 + 16 * gw) * 4096;
    g.wstr = 2 * 4096;
    g.sw = sw0 + rot * 512;
    const unsigned wdst = lds0 + L_W + 8 * gw * 1024;
    if (HASG) { g.dw = wdst; w_all<0>(g); asm volatile("s_waitcnt vmcnt(8)" ::: "memory"); }
    else WAIT_VM(0);
    unsigned* flags = (unsigned*)(ws + OFF_FLAG);
    if (lane == 0) {
        const unsigned old = __hip_atomic_fetch_add((LDSP unsigned*)(lds + L_RED) + C_PUB, 1u, __ATOMIC_RELAXED, __HIP_MEMORY_SCOPE_WORKGROUP);
        if (old == 3u) __hip_atomic_store(flags + (b >> 2) * 32 + (b & 3), 1u, __ATOMIC_RELAXED, __HIP_MEMORY_SCOPE_AGENT);
    }
    STAMP(5);
    const int ph = gw >> 1;
    const unsigned kvinc = (gw & 1) ? 4 * VT_BYTES : 4 * KT_BYTES;
    {
        const unsigned base = ((gw & 1) ? (unsigned)(OFF_VF - OFF_KF) + (ph * 32) * VT_BYTES : (ph * 32 + 1) * KT_BYTES) + 3 * kvinc;
#pragma unroll
        for (int i = 0; i < 6; ++i) g.skv[i] = base + i * 1024;
    }
    const unsigned kvdst = lds0 + L_KV + 6 * gw * 1024;
    if constexpr (!HASG) {
        lds_add(lds, C_GAT);
        unsigned tw = 0;
        const unsigned vpw = (lane >> 2) * 4096 + (lane & 3) * 128, spw = (unsigned)((b & 7) * 256 + (((b >> 3) - 28) * 4 + gw) * 16) * 4096;
        touch_line(tw, g.rw, vpw, spw + 512);
        int buf = 0;
#pragma unroll 1
        for (int m = 0; m < 6; ++m) {
            lds_wait_ge(lds, C_ATT + m, 8u);
            WAIT_VM(0); TOUCH_DONE(tw);
            touch_line(tw, g.rw, vpw, spw + (m + 2) * 512);
            if (m < 5) {
                g.dkv = kvdst + buf * KV_STAGE;
                kv_all<0>(g);
#pragma unroll
                for (int i = 0; i < 6; ++i) g.skv[i] += kvinc;
            }
            WAIT_VM(0);
            if (m < 5) lds_add(lds, C_GAT);
            buf = (buf == 2) ? 0 : buf + 1;
        }
        TOUCH_DONE(tw);
        lds_wait_ge(lds, C_ATT + K2_PFSS, 8u);
        {
            const int part = ((b >> 3) - 28) * 4 + gw;
            const unsigned colb = (lane % 10) * 128u, rsub = min(lane / 10, 5);
            const rsrc_t rr = part < 7 ? mk_rsrc(P.wo, D_ * 1280) : mk_rsrc(P.wout, FH_ * 1280);
            const unsigned r0 = (unsigned)((part < 7 ? part : part - 7) * 96) * 1280;
            touch_lines8(rr, rsub * 1280 + colb, r0, 6 * 1280);
            touch_lines8(rr, rsub * 1280 + colb, r0 + 48 * 1280, 6 * 1280);
            touch_lines8(mk_rsrc(P.x, S_ * 1280), min(rsub, 1u) * 1280 + colb, (unsigned)((b & 7) * 256 + part * 16) * 1280, 2 * 1280);
        }
        return;
    }
    g.zoff = (h * FH_ + f0 + r31) * 16;
    g.g0 = f32x16{}; g.g1 = f32x16{};
#pragma unroll
    for (int i = 0; i < 16; ++i) g.z[i] = u32x4{0, 0, 0, 0};
    lds_ptr wb = lds + L_W;
    const unsigned wx = r31 * 512 + (((lane & 15) ^ h) << 4);
    if (gw == 0) {
        const u32x4* fp = (const u32x4*)flags + lane * 8;
        for (unsigned spin = 0; spin < (1u << 22); ++spin) {
            __builtin_amdgcn_s_sleep(K2_POLL);
            u32x4 f;
            asm volatile("global_load_dwordx4 %0, %1, off sc1\n\ts_waitcnt vmcnt(0)" : "=&v"(f) : "v"(fp) : "memory");
            if (__all(f[0] == 1u && f[1] == 1u && f[2] == 1u && f[3] == 1u)) break;
        }
        lds_add(lds, C_FLAG);
    } else lds_wait_ge(lds, C_FLAG, 1u);
    STAMP(6);
    WAIT_VM(0);
    lds_add(lds, C_GAT);
    __builtin_amdgcn_s_setprio(GSTEP_PRIO);
    z_loads(g, rot * 16 * ZSTEP);
    int buf = 0;
    auto stage_head = [&](int m) {
        lds_wait_ge(lds, C_GAT, 4u * (m + 1));
        if (m < 5) lds_wait_ge(lds, C_ATT + m, 8u);
        if (m == 5) g.rkv = mk_rsrc(ws, 0);
        g.dkv = (m < 5) ? kvdst + buf * KV_STAGE : lds0 + L_DUMMY; g.dw = wdst + ((m + 1) & 1) * W_STAGE;
        g.sw = sw0 + ((m + 1 + rot) & 7) * 512;
        if (m == 7) {
            g.rz = mk_rsrc(ws, 0);
            g.rw = mk_rsrc(ws + OFF_Z1, S_ * 1728); g.sw = (t0 + 16 * gw) * 1728 + (f0 >> 7) * 256; g.wstr = 4 * 1728;
#pragma unroll
            for (int i = 0; i < 8; ++i) g.vw[i] = i < 4 ? (lane >> 4) * 1728 + (((lane & 15) ^ ((4 * i + (lane >> 4)) & 15)) << 4) : 0x7f000000u;
        }
    };
    auto stage_tail = [&]() {
#pragma unroll
        for (int i = 0; i < 6; ++i) g.skv[i] += kvinc;
        asm volatile("s_waitcnt vmcnt(6) lgkmcnt(0)" ::: "memory");
        lds_add(lds, C_GAT);
        buf = (buf == 2) ? 0 : buf + 1;
    };
    stage_head(0); g_step<0, true>(g, wb, wx, ((1 + rot) & 7) * 16 * ZSTEP); stage_tail();
#pragma unroll 1
    for (int m = 1; m < 8; ++m) { stage_head(m); g_step<0, false>(g, wb + (m & 1) * W_STAGE, wx, ((m + 1 + rot) & 7) * 16 * ZSTEP); stage_tail(); }
    asm volatile("s_waitcnt vmcnt(0)" : Z_TIE(g) :: "memory");
    STAMP(15);
    lds_wait_ge(lds, C_GAT, 36u);
#pragma unroll
    for (int u = 0; u < 2; ++u) {
        const int r = 32 * u + r31;
        const float bias = bf2f(P.bsp[t0 + r]);
        const f32x16& gg = u ? g.g1 : g.g0;
#pragma unroll
        for (int i = 0; i < 4; ++i) {
            const int ch = (4 * gw + i) ^ (r & 15);
            u32x2 o = u32x2{0u, 0u};
            if (f0 < FH_) {
                const u32x2 z1 = *(const LDSP u32x2*)(lds + L_W + (r >> 4) * 8192 + (r & 15) * 256 + ch * 16 + h * 8);
                o = u32x2{pk2(bflo(z1[0]) * (gg[4 * i] + bias), bfhi(z1[0]) * (gg[4 * i + 1] + bias)),
                          pk2(bflo(z1[1]) * (gg[4 * i + 2] + bias), bfhi(z1[1]) * (gg[4 * i + 3] + bias))};
            }
            *(LDSP u32x2*)(lds + L_W + W_STAGE + r * 256 + ch * 16 + h * 8) = o;
        }
    }
    asm volatile("s_waitcnt lgkmcnt(0)" ::: "memory");
    lds_add(lds, C_UST);
    lds_wait_ge(lds, C_UST, 4u);
    const rsrc_t rou = mk_rsrc(ws + OFF_OU, S_ * 3072);
#pragma unroll
    for (int i = 0; i < 4; ++i) {
        const int r = 16 * gw + 4 * i + (lane >> 4), c = lane & 15;
        st128(rou, (unsigned)((t0 + r) * 3072 + 1280 + (f0 >> 7) * 256 + c * 16), ldsr128(lds + L_W + W_STAGE + r * 256 + ((c ^ (r & 15)) << 4)));
    }
}

__global__ __launch_bounds__(768, 3) void k2_mix(Params P) {
    extern __shared__ __attribute__((aligned(16))) char smem[];
    const int lane = threadIdx.x & 63, w = __builtin_amdgcn_readfirstlane(threadIdx.x >> 6);
    STAMP_DECL; STAMP(0); RSTAMP(30);
    const int b = blockIdx.x, j = b >> 3;
    lds_ptr lds = (lds_ptr)smem; const unsigned lds0 = (unsigned)(uintptr_t)smem;
    if (w < 8) attention(P, lds, lds0, b, w, lane STAMP_PASS);
    else if (j < 28) { const int rbg = (b & 7) * 4 + j / 7, cbg = j % 7; gating<true>(P, lds, lds0, b, w - 8, lane, rbg * 64, cbg * 128 + (w - 8) * 32 STAMP_PASS); }
    else gating<false>(P, lds, lds0, b, w - 8, lane, 0, 0 STAMP_PASS);
    STAMP(17); RSTAMP(31); STAMP_FLUSH_K(1);
}
}
namespace k3 {
#define K3_AUX 16
constexpr int BM = 96, A_BYTES = BM * 128, STAGE = 20480, NST = 7, NKS = 24, LDS_BYTES = NST * STAGE;
struct Params { const u16* x; const u16* wo; const u16* wout; const char* ws; u16* out; };
struct Ld { rsrc_t ra, rb0, rb1, rx; unsigned va[2], vb[2], sa, sb, da, db, vx, sx; int rot; };
constexpr int L_X = 3 * STAGE;
template <int S> DI void ld_issue(const Ld& L) {
    constexpr int so = (S % NST) * STAGE;
    int gs = S + L.rot; if (gs >= NKS) gs -= NKS;
    const bool lo = gs < 10;
    const rsrc_t rb = lo ? L.rb0 : L.rb1;
    const unsigned sb = L.sb + (unsigned)(lo ? gs : gs - 10) * (64 * 1280);
#pragma unroll
    for (int j = 0; j < 3; ++j) bdma(L.ra, L.va[j & 1], L.sa + gs * 128 + j * 8 * 3072, L.da + so + j * 1024);
#pragma unroll
    for (int j = 0; j < 2; ++j) bdma(rb, L.vb[j], sb + j * 8 * 1280, L.db + so + j * 1024);
}
constexpr int issued_at(int S) { return (S < 3 ? 2 + 2 * S : S + 5) < NKS ? (S < 3 ? 2 + 2 * S : S + 5) : NKS - 1; }
template <int A, int B> DI void ld_range(const Ld& L) { if constexpr (A <= B) { ld_issue<A>(L); ld_range<A + 1, B>(L); } }
template <int S> DI void ld_step(const Ld& L) {
    if constexpr (S > 0) ld_range<issued_at(S - 1) + 1, issued_at(S)>(L);
    if constexpr (S == 19) {
#pragma unroll
        for (int j = 0; j < 3; ++j) bdma(L.rx, L.vx, L.sx + j * 8 * 1280, L.da + L_X + j * 1024);
    }
    constexpr int young = 5 * (issued_at(S) - (S + 1)) + (S >= 19 && S < NKS - 2 ? 3 : 0);
    asm volatile("s_waitcnt vmcnt(%0) lgkmcnt(0)\n\ts_barrier" :: "i"(young) : "memory");
    if constexpr (S + 2 < NKS) ld_step<S + 1>(L);
}
struct Frag { u32x4 x[3], w[2]; };
struct Ctx { unsigned xo[3], wo0[3], wo1[3]; };
template <int S> DI void rd(Frag& f, lds_ptr lds, const Ctx& c) {
    constexpr int slot = S % NST, grp = slot / 3, so = (slot % 3) * STAGE;
#pragma unroll
    for (int n = 0; n < 2; ++n) {
        lds_ptr wp = lds + (n ? c.wo1[grp] : c.wo0[grp]) + so;
        const u32x2 lo = ldstr(wp), hi = ldstr(wp + 512);
        f.w[n] = u32x4{lo[0], lo[1], hi[0], hi[1]};
    }
#pragma unroll
    for (int m = 0; m < 3; ++m) f.x[m] = ldsr128(lds + c.xo[grp] + so + m * 2048);
}
template <int S> DI void stage(lds_ptr lds, const Ctx& c, Frag& fc, Frag& fn, f32x4 (&acc)[3][2] STAMP_ARG) {
    if constexpr (S + 1 < NKS) {
        BAR_LDS();
        STAMP(2 + S);
        rd<S + 1>(fn, lds, c);
        SBAR();
    }
#pragma unroll
    for (int m = 0; m < 3; ++m)
#pragma unroll
        for (int n = 0; n < 2; ++n) acc[m][n] = mfma16(fc.w[n], fc.x[m], acc[m][n]);
    SBAR();
    if constexpr (S + 1 < NKS) stage<S + 1>(lds, c, fn, fc, acc STAMP_PASS);
}

__global__ __launch_bounds__(768, 3) void k3_out(Params P) {
    extern __shared__ __attribute__((aligned(16))) char smem[];
    const int tid = threadIdx.x, lane = tid & 63, w = __builtin_amdgcn_readfirstlane(tid >> 6);
    STAMP_DECL; STAMP(0); RSTAMP(30);
    const int b = blockIdx.x, j0 = b >> 3;
    if (j0 >= 30) return;
    const int rbl = j0 / 10, cb = j0 - rbl * 10;
    const int row0 = (b & 7) * 256 + rbl * BM, rowlim = (b & 7) * 256 + 256;
    lds_ptr lds = (lds_ptr)smem;
    const unsigned lds0 = (unsigned)(uintptr_t)smem;
    if (w >= 8) {
        const int L = w - 8, r8 = lane >> 3, c8 = lane & 7, k4 = lane >> 4;
        Ld ld;
        ld.ra = mk_rsrc(P.ws + OFF_OU, rowlim * 3072); ld.rb0 = mk_rsrc(P.wo, D_ * 1280); ld.rb1 = mk_rsrc(P.wout, FH_ * 1280);
#pragma unroll
        for (int i = 0; i < 2; ++i) {
            ld.va[i] = r8 * 3072 + ((c8 ^ ((4 * ((L + i) & 1) + k4) & 7)) << 4);
            ld.vb[i] = r8 * 1280 + ((c8 ^ (((k4 & 1) | (i << 1)) << 1)) << 4);
        }
        ld.sa = (row0 + 24 * L) * 3072; ld.sb = cb * 128 + 16 * L * 1280;
        ld.da = lds0 + 3 * L * 1024; ld.db = lds0 + A_BYTES + 2 * L * 1024;
        ld.rx = mk_rsrc(P.x, rowlim * 1280); ld.vx = r8 * 1280 + c8 * 16; ld.sx = (row0 + 24 * L) * 1280 + cb * 128;
        ld.rot = 0;
        STAMP(1);
        ld_issue<0>(ld); ld_issue<1>(ld); ld_issue<2>(ld);
        STAMP(2);
        WAIT_BAR(10);
        STAMP(3);
        ld_step<0>(ld);
        return;
    }
    const int kh = w >> 2, wr = w & 1, wc = (w >> 1) & 1;
    const int r15 = lane & 15, g = lane >> 4, q = r15 >> 2, p = lane & 3;
    Ctx c;
    const unsigned xoff = (48 * wr + r15) * 128 + (((4 * kh + g) ^ ((r15 >> 1) & 7)) << 4);
    const int fk = (((q >> 1) & 1) | ((g & 1) << 1)) << 1;
    const unsigned woff = A_BYTES + (32 * kh + 8 * g + q) * 128 + (((4 * wc + (p >> 1)) ^ fk) << 4) + 8 * (p & 1);
#pragma unroll
    for (int i = 0; i < 3; ++i) { c.xo[i] = xoff + i * 3 * STAGE; c.wo0[i] = woff + i * 3 * STAGE; c.wo1[i] = (woff ^ 32) + i * 3 * STAGE; }
    f32x4 acc[3][2];
#pragma unroll
    for (int m = 0; m < 3; ++m) { acc[m][0] = f32x4{0, 0, 0, 0}; acc[m][1] = f32x4{0, 0, 0, 0}; }
    Frag f0, f1;
    BAR_LDS();
    rd<0>(f0, lds, c);
    const int co = cb * 64 + wc * 32;

    stage<0>(lds, c, f0, f1, acc STAMP_PASS);
    STAMP(28);
    BAR_LDS();
    STAMP(25);
    LDSP f32x4* xch = (LDSP f32x4*)lds + (w & 3) * 6 * 64;
    if (kh == 1) {
#pragma unroll
        for (int m = 0; m < 2; ++m) { xch[(m * 2) * 64 + lane] = acc[m][0]; xch[(m * 2 + 1) * 64 + lane] = acc[m][1]; }
    } else { xch[4 * 64 + lane] = acc[2][0]; xch[5 * 64 + lane] = acc[2][1]; }
    BAR_LDS();
    STAMP(26);
#pragma unroll
    for (int u = 0; u < 2; ++u) {
        if (kh == 1 && u == 1) break;
        const int m = kh ? 2 : u;
        const int tb = row0 + 48 * wr + 16 * m;
        if (tb >= rowlim) break;
        const int t = tb + r15;
        unsigned pk[2][2];
#pragma unroll
        for (int n = 0; n < 2; ++n) {
            const f32x4 mine = kh ? acc[2][n] : acc[u][n];
            const f32x4 v = mine + xch[(m * 2 + n) * 64 + lane];
            const u32x2 xv = *(const LDSP u32x2*)(lds + L_X + (48 * wr + 16 * m + r15) * 128 + (wc * 32 + 16 * n + 4 * g) * 2);
            pk[n][0] = pk2(v[0] + bflo(xv[0]), v[1] + bfhi(xv[0])); pk[n][1] = pk2(v[2] + bflo(xv[1]), v[3] + bfhi(xv[1]));
        }
        auto x0 = __builtin_amdgcn_permlane16_swap(pk[0][0], pk[1][0], false, false);
        auto x1 = __builtin_amdgcn_permlane16_swap(pk[0][1], pk[1][1], false, false);
        __builtin_amdgcn_raw_buffer_store_b128(u32x4{x0[0], x1[0], x0[1], x1[1]}, mk_rsrc(P.out, S_ * 1280), (unsigned)(t * D_ + co + 16 * (g & 1) + 8 * (g >> 1)) * 2, 0, K3_AUX);
    }
    STAMP(29); RSTAMP(31); STAMP_FLUSH_K(2);
}
}
extern "C" void kernel_launch(void* const* d_in, const int* in_sizes, int n_in, void* d_out, int out_size, void* d_ws, size_t ws_size,
                              hipStream_t stream) {
    (void)in_sizes; (void)n_in; (void)out_size; (void)ws_size;
    const u16* x = (const u16*)d_in[0];
    k1::Params p1{x, (const u16*)d_in[1], (const u16*)d_in[2], (const u16*)d_in[3], (const u16*)d_in[6], (const u16*)d_in[7], (char*)d_ws};
    k2::Params p2{(const u16*)d_in[7], (const u16*)d_in[8], (const u16*)d_in[4], (const u16*)d_in[9], x, (char*)d_ws};
    k3::Params p3{x, (const u16*)d_in[4], (const u16*)d_in[9], (const char*)d_ws, (u16*)d_out};
    (void)hipFuncSetAttribute((const void*)k1::k1_proj, hipFuncAttributeMaxDynamicSharedMemorySize, k1::LDS_BYTES);
    (void)hipFuncSetAttribute((const void*)k2::k2_mix, hipFuncAttributeMaxDynamicSharedMemorySize, k2::LDS_BYTES);
    (void)hipFuncSetAttribute((const void*)k3::k3_out, hipFuncAttributeMaxDynamicSharedMemorySize, k3::LDS_BYTES);
    k1::k1_proj<<<256, 768, k1::LDS_BYTES, stream>>>(p1);
    k2::k2_mix<<<256, 768, k2::LDS_BYTES, stream>>>(p2);
    k3::k3_out<<<256, 768, k3::LDS_BYTES, stream>>>(p3);
}
```

```cpp
#include <hip/hip_runtime.h>
#include <stdint.h>
#pragma clang diagnostic ignored "-Winline-asm"

typedef unsigned short u16;
using bf16x8 = __attribute__((ext_vector_type(8))) __bf16;
using bf16x2 = __attribute__((ext_vector_type(2))) __bf16;
using s16x4 = __attribute__((ext_vector_type(4))) short;
using f32x2 = __attribute__((ext_vector_type(2))) float;
using f32x4 = __attribute__((ext_vector_type(4))) float;
using f32x16 = __attribute__((ext_vector_type(16))) float;
using u32x2 = __attribute__((ext_vector_type(2))) unsigned;
using u32x4 = __attribute__((ext_vector_type(4))) unsigned;
#define DI __device__ __forceinline__
#define LDSP __attribute__((address_space(3)))
typedef LDSP char* lds_ptr;

constexpr int S_ = 2048, D_ = 640, FH_ = 864, F_ = 1728;
constexpr size_t OFF_Q = 0;
constexpr size_t OFF_KF = OFF_Q + (size_t)S_ * D_ * 2;
constexpr size_t OFF_VF = OFF_KF + 131072;
constexpr size_t OFF_Z1 = OFF_VF + 131072;
constexpr size_t OFF_Z2R = OFF_Z1 + (size_t)S_ * FH_ * 2;
constexpr size_t OFF_Z2N = OFF_Z2R + (size_t)S_ * FH_ * 2;
constexpr size_t OFF_OU = OFF_Z2N + (size_t)S_ * FH_ * 2 + 4096;
constexpr size_t OFF_FLAG = OFF_OU + (size_t)S_ * 1536 * 2;
constexpr int V_SLOTS = 22;
constexpr int KT_BYTES = 1536, VT_BYTES = 4 * V_SLOTS * 16;

DI float bf2f(u16 b) { return __uint_as_float(((unsigned)b) << 16); }
DI unsigned pk2(float lo, float hi) { bf16x2 v = {(__bf16)lo, (__bf16)hi}; return __builtin_bit_cast(unsigned, v); }
DI float bflo(unsigned v) { return __uint_as_float(v << 16); }
DI float bfhi(unsigned v) { return __uint_as_float(v & 0xffff0000u); }
DI void glds16(const void* g, unsigned lds_base) {
    unsigned sv;
    asm volatile("s_mov_b32 %0, m0\n\ts_mov_b32 m0, %2\n\ts_nop 0\n\tglobal_load_lds_dwordx4 %1, off\n\ts_mov_b32 m0, %0"
                 : "=&s"(sv) : "v"(g), "s"(lds_base) : "memory");
}
DI void glds(const void* g, unsigned lds_dst) {
    asm volatile("s_mov_b32 m0, %1\n\ts_nop 0\n\tglobal_load_lds_dwordx4 %0, off" :: "v"(g), "s"(lds_dst) : "memory", "m0");
}
DI void glds_lo32(const void* g, unsigned lds_dst) {
    asm volatile("s_mov_b32 m0, %1\n\ts_mov_b32 exec_hi, 0\n\tglobal_load_lds_dwordx4 %0, off\n\ts_mov_b32 exec_hi, -1" :: "v"(g), "s"(lds_dst) : "memory", "m0");
}
typedef __amdgpu_buffer_rsrc_t rsrc_t;
DI rsrc_t mk_rsrc(const void* p, unsigned bytes) { return __builtin_amdgcn_make_buffer_rsrc((void*)p, 0, bytes, 0x00020000); }
DI void bdma(rsrc_t r, unsigned voff, unsigned soff, unsigned lds_dst) {
    asm volatile("s_mov_b32 m0, %3\n\ts_nop 0\n\tbuffer_load_dwordx4 %0, %1, %2 offen lds" :: "v"(voff), "s"(r), "s"(soff), "s"(lds_dst) : "memory", "m0");
}
#define WAIT_VM(n) asm volatile("s_waitcnt vmcnt(" #n ")" ::: "memory")
#define WAIT_BAR(n) asm volatile("s_waitcnt vmcnt(" #n ") lgkmcnt(0)\n\ts_barrier" ::: "memory")
#define BAR_LDS() asm volatile("s_waitcnt lgkmcnt(0)\n\ts_barrier" ::: "memory")
#define SBAR() __builtin_amdgcn_sched_barrier(0)
DI u32x4 ldsr128(lds_ptr p) { return *(const LDSP u32x4*)p; }
typedef short v4i16_t __attribute__((ext_vector_type(4)));
DI u32x2 ldstr(lds_ptr p) { return __builtin_bit_cast(u32x2, __builtin_amdgcn_ds_read_tr16_b64_v4i16((LDSP v4i16_t*)p)); }
#define ST_AUX 16
DI void st128(rsrc_t r, unsigned off, u32x4 v) { __builtin_amdgcn_raw_buffer_store_b128(v, r, off, 0, ST_AUX); }
DI void st64(rsrc_t r, unsigned off, u32x2 v) { __builtin_amdgcn_raw_buffer_store_b64(v, r, off, 0, ST_AUX); }
DI void touch_lines8(rsrc_t r, unsigned voff, unsigned soff, unsigned step) {
    unsigned t0, t1, t2, t3, t4, t5, t6, t7;
    asm volatile("buffer_load_dword %0, %8, %9, %10 offen\n\tbuffer_load_dword %1, %8, %9, %11 offen\n\tbuffer_load_dword %2, %8, %9, %12 offen\n\t"
                 "buffer_load_dword %3, %8, %9, %13 offen\n\tbuffer_load_dword %4, %8, %9, %14 offen\n\tbuffer_load_dword %5, %8, %9, %15 offen\n\t"
                 "buffer_load_dword %6, %8, %9, %16 offen\n\tbuffer_load_dword %7, %8, %9, %17 offen\n\ts_waitcnt vmcnt(0)"
                 : "=&v"(t0), "=&v"(t1), "=&v"(t2), "=&v"(t3), "=&v"(t4), "=&v"(t5), "=&v"(t6), "=&v"(t7)
                 : "v"(voff), "s"(r), "s"(soff), "s"(soff + step), "s"(soff + 2 * step), "s"(soff + 3 * step), "s"(soff + 4 * step), "s"(soff + 5 * step),
                   "s"(soff + 6 * step), "s"(soff + 7 * step) : "memory");
}
DI void touch_line(unsigned& t, rsrc_t r, unsigned voff, unsigned soff) {
    asm volatile("buffer_load_dword %0, %1, %2, %3 offen" : "=v"(t) : "v"(voff), "s"(r), "s"(soff) : "memory");
}
#define TOUCH_DONE(t) asm volatile("" :: "v"(t))
DI f32x4 mfma16(u32x4 a, u32x4 b, f32x4 c) { return __builtin_amdgcn_mfma_f32_16x16x32_bf16(__builtin_bit_cast(bf16x8, a), __builtin_bit_cast(bf16x8, b), c, 0, 0, 0); }
DI void mfma16_acc(f32x4& acc, const u32x4& a, const u32x4& b) { asm volatile("v_mfma_f32_16x16x32_bf16 %0, %1, %2, %0" : "+v"(acc) : "v"(a), "v"(b)); }
DI f32x16 mfma32(u32x4 a, u32x4 b, f32x16 c) { return __builtin_amdgcn_mfma_f32_32x32x16_bf16(__builtin_bit_cast(bf16x8, a), __builtin_bit_cast(bf16x8, b), c, 0, 0, 0); }
DI f32x16 mfma32k8(u32x2 a, u32x2 b, f32x16 c) { return __builtin_amdgcn_mfma_f32_32x32x8bf16_1k(__builtin_bit_cast(s16x4, a), __builtin_bit_cast(s16x4, b), c, 0, 0, 0); }
DI float dot2(unsigned a, unsigned b, float c) { return __builtin_amdgcn_fdot2_f32_bf16(__builtin_bit_cast(bf16x2, a), __builtin_bit_cast(bf16x2, b), c, false); }
DI float gelu_tanh(float x) {
    const float c0 = -2.302208198f, c1 = -0.1029432397f;
    float e = __builtin_amdgcn_exp2f(x * __builtin_fmaf(x * x, c1, c0));
    return x * __builtin_amdgcn_rcpf(1.f + e);
}
#define STAMP_DECL
#define STAMP(i)
#define RSTAMP(i)
#define STAMP_FLUSH_K(k)
#define STAMP_ARG
#define STAMP_PASS
namespace k1 {
#define K1_SPARE_PF 0
#define K1_EPRIO 1
#define USE1(x)
constexpr int BM = 160, A_BYTES = BM * 128, B_BYTES = 64 * 256, NKS = 10;
constexpr int L_A = 0, L_B = 4 * A_BYTES, L_ST = L_B + 4 * B_BYTES, LDS_BYTES = L_ST + 4096;
enum { T_Q = 0, T_K = 1, T_V = 2, T_Z1 = 3, T_Z2 = 4 };
struct Params { const u16* x; const u16* wq; const u16* wk; const u16* wv; const u16* win; const u16* wsp; char* ws; };

struct Ld { rsrc_t ra, rb, rp; unsigned va[2], vb[2], sa, sb, sbinc, da, db, vp, sp, tp; const char* pk[4]; unsigned pinc[4]; int rot; };
#define K1_PFS 99
template <bool KV, int S> DI void ld_issue(Ld& L) {
    int gs = S + L.rot; if (gs >= NKS) gs -= NKS;
#pragma unroll
    for (int j = 0; j < 5; ++j) bdma(L.ra, L.va[j & 1], L.sa + gs * 128 + j * 8 * 1280, L.da + (S & 3) * A_BYTES + j * 1024);
#pragma unroll
    for (int j = 0; j < 4; ++j) {
        if constexpr (KV) glds(L.pk[j] + (unsigned)gs * L.pinc[j], L.db + (S & 3) * B_BYTES + j * 1024);
        else bdma(L.rb, L.vb[(j >> 1) & 1], L.sb + gs * L.sbinc + j * (L.sbinc >> 4), L.db + (S & 3) * B_BYTES + j * 1024);
    }
}
template <bool KV, int S> DI void ld_step(Ld& L) {
    if constexpr (S == 1) { ld_issue<KV, 2>(L); ld_issue<KV, 3>(L); }
    else if constexpr (S >= 2 && S + 2 < NKS) ld_issue<KV, S + 2>(L);
    if constexpr (S == K1_PFS) touch_line(L.tp, L.rp, L.vp, L.sp);
    constexpr int issued = S == 0 ? 1 : (S + 2 < NKS ? S + 2 : NKS - 1);
    asm volatile("s_waitcnt vmcnt(%0) lgkmcnt(0)\n\ts_barrier" :: "i"(9 * (issued - (S + 1)) + (S == K1_PFS ? 1 : 0)) : "memory");
    if constexpr (S == K1_PFS + 1) TOUCH_DONE(L.tp);
    if constexpr (S + 2 < NKS) ld_step<KV, S + 1>(L);
}
template <bool KV> DI void loader(Ld& L STAMP_ARG) {
    ld_issue<KV, 0>(L); ld_issue<KV, 1>(L);
    STAMP(2);
    WAIT_BAR(9);
    STAMP(3);
    ld_step<KV, 0>(L);
}

struct Frag { u32x4 x[5], w[2]; };
template <int S, int KS> DI void rd(Frag& f, lds_ptr lds, const unsigned (&xo)[5], unsigned wo0, unsigned wo1) {
    constexpr int sa = (S & 3) * A_BYTES, sb = (S & 3) * B_BYTES + KS * 8192;
#pragma unroll
    for (int n = 0; n < 2; ++n) {
        lds_ptr wp = lds + (n ? wo1 : wo0) + sb;
        const u32x2 lo = ldstr(wp), hi = ldstr(wp + 1024);
        f.w[n] = u32x4{lo[0], lo[1], hi[0], hi[1]};
    }
#pragma unroll
    for (int m = 0; m < 5; ++m) f.x[m] = ldsr128(lds + (xo[m] ^ (KS * 64)) + sa);
}
struct Stat { f32x4 s0, q0, s1, q1, cs; };
template <int S, int KS, int I> DI void rd1(Frag& f, lds_ptr lds, const unsigned (&xo)[5], unsigned wo0, unsigned wo1) {
    constexpr int sa = (S & 3) * A_BYTES, sb = (S & 3) * B_BYTES + KS * 8192;
    if constexpr (I < 4) {
        const u32x2 t = ldstr(lds + ((I >> 1) ? wo1 : wo0) + sb + (I & 1) * 1024);
        f.w[I >> 1][2 * (I & 1)] = t[0]; f.w[I >> 1][2 * (I & 1) + 1] = t[1];
    } else f.x[I - 4] = ldsr128(lds + (xo[I - 4] ^ (KS * 64)) + sa);
}
template <bool FT, int KSG, bool RD, int S, int KS>
DI void mm(const Frag& fc, Frag& fn, lds_ptr lds, const unsigned (&xo)[5], unsigned wo0, unsigned wo1, f32x4 (&acc)[5][2], int wc, Stat& st, const u32x4& ones) {
#define K1_MM(i) do { if (FT) mfma16_acc(acc[(i) >> 1][(i) & 1], fc.w[(i) & 1], fc.x[(i) >> 1]); else mfma16_acc(acc[(i) >> 1][(i) & 1], fc.x[(i) >> 1], fc.w[(i) & 1]); } while (0)
#define K1_RD(i) do { if constexpr (RD) rd1<S, KS, i>(fn, lds, xo, wo0, wo1); SBAR(); } while (0)
    K1_MM(0); K1_RD(0); K1_MM(1); K1_RD(1); K1_MM(2); K1_RD(2); K1_MM(3); K1_RD(3); K1_MM(4); K1_RD(4);
    K1_MM(5); K1_RD(5); K1_MM(6); K1_RD(6); K1_MM(7); K1_RD(7); K1_MM(8); K1_RD(8); K1_MM(9);
#undef K1_MM
#undef K1_RD
    mfma16_acc(st.s0, fc.x[0], ones); mfma16_acc(st.q0, fc.x[0], fc.x[0]); mfma16_acc(st.cs, ones, fc.w[0]);
    if ((KSG & 3) == wc) { mfma16_acc(st.s1, fc.x[4], ones); mfma16_acc(st.q1, fc.x[4], fc.x[4]); }
    SBAR();
}
template <bool FT, int S>
DI void stage(lds_ptr lds, Frag& f0, Frag& f1, const unsigned (&xo)[5], unsigned wo0, unsigned wo1, int wc,
              f32x4 (&acc)[5][2], Stat& st, const u32x4& ones STAMP_ARG) {
    if constexpr (S + 1 < NKS) BAR_LDS();
    STAMP(3 + S);
    mm<FT, 2 * S, true, S, 1>(f0, f1, lds, xo, wo0, wo1, acc, wc, st, ones);
    mm<FT, 2 * S + 1, (S + 1 < NKS), S + 1, 0>(f1, f0, lds, xo, wo0, wo1, acc, wc, st, ones);
    if constexpr (S + 1 < NKS) stage<FT, S + 1>(lds, f0, f1, xo, wo0, wo1, wc, acc, st, ones STAMP_PASS);
}
template <bool FT>
DI void mainloop(lds_ptr lds, int lane, int wr, int wc, f32x4 (&acc)[5][2], Stat& st STAMP_ARG) {
    const int r15 = lane & 15, g = lane >> 4;
    const unsigned xb = L_A + (80 * wr + r15) * 128 + ((g ^ ((r15 >> 1) & 7)) << 4);
    unsigned xo[5];
#pragma unroll
    for (int m = 0; m < 4; ++m) xo[m] = xb + (m ^ wc) * 2048;
    xo[4] = xb + 8192;
    const int q = r15 >> 2, p = lane & 3;
    const int fk = (q << 1) | ((g & 1) << 3);
    const unsigned wo0 = (L_B + (8 * g + q) * 256 + (((4 * wc + (p >> 1)) ^ fk) << 4) + 8 * (p & 1)) ^ (wr * 32), wo1 = wo0 ^ 32;
    Frag f0, f1;
    u32x4 ones = u32x4{0x3F803F80u, 0x3F803F80u, 0x3F803F80u, 0x3F803F80u};
    asm volatile("" : "+v"(ones), "+v"(st.s0), "+v"(st.q0), "+v"(st.s1), "+v"(st.q1), "+v"(st.cs));
#pragma unroll
    for (int m = 0; m < 5; ++m) asm volatile("" : "+v"(acc[m][0]), "+v"(acc[m][1]));
    BAR_LDS();
    rd<0, 0>(f0, lds, xo, wo0, wo1);
    stage<FT, 0>(lds, f0, f1, xo, wo0, wo1, wc, acc, st, ones STAMP_PASS);
    asm volatile("s_nop 7\n\ts_nop 7" ::: "memory");
}
template <int N> DI void gelu_n(f32x2* v) {
    f32x2 t[N];
#pragma unroll
    for (int i = 0; i < N; ++i) t[i] = v[i] * __builtin_elementwise_fma(v[i] * v[i], f32x2{-0.1029432397f, -0.1029432397f}, f32x2{-2.302208198f, -2.302208198f});
    SBAR();
#pragma unroll
    for (int i = 0; i < N; ++i) t[i] = f32x2{__builtin_amdgcn_exp2f(t[i][0]), __builtin_amdgcn_exp2f(t[i][1])};
    SBAR();
#pragma unroll
    for (int i = 0; i < N; ++i) t[i] += f32x2{1.f, 1.f};
#pragma unroll
    for (int i = 0; i < N; ++i) t[i] = f32x2{__builtin_amdgcn_rcpf(t[i][0]), __builtin_amdgcn_rcpf(t[i][1])};
    SBAR();
#pragma unroll
    for (int i = 0; i < N; ++i) v[i] *= t[i];
}
template <int TYPE>
DI void epilogue(char* ws, const f32x4 (&acc)[5][2], const LDSP float* csum, const LDSP float* ra, const LDSP float* rbv, int tw, int col0, int lane, int wr, int wc STAMP_ARG) {
    const int r15 = lane & 15, g = lane >> 4;
    constexpr bool FT = (TYPE == T_Q || TYPE == T_K || TYPE == T_Z1);
    float cf[2][4];
#pragma unroll
    for (int n = 0; n < 2; ++n) {
        if constexpr (FT) { const f32x4 c4 = *(const LDSP f32x4*)(csum + 32 * wc + 16 * (n ^ wr) + 4 * g); for (int j = 0; j < 4; ++j) cf[n][j] = c4[j]; }
        else cf[n][0] = csum[32 * wc + 16 * (n ^ wr) + r15];
    }
    f32x4 av[5], bv[5];
#pragma unroll
    for (int mp = 0; mp < 5; ++mp) {
        const int m = mp < 4 ? (mp ^ wc) : 4;
        if constexpr (FT) { av[mp][0] = ra[80 * wr + 16 * m + r15]; bv[mp][0] = rbv[80 * wr + 16 * m + r15]; }
        else { av[mp] = *(const LDSP f32x4*)(ra + 80 * wr + 16 * m + 4 * g); bv[mp] = *(const LDSP f32x4*)(rbv + 80 * wr + 16 * m + 4 * g); }
    }
    USE1(av[4][0]); USE1(bv[4][0]); STAMP(16);
    const rsrc_t rws = mk_rsrc(ws, (unsigned)OFF_Z2N);
#pragma unroll
    for (int mp = 0; mp < 5; ++mp) {
        const int m = mp < 4 ? (mp ^ wc) : 4;
        const int tb = tw + 16 * m;
        if (((wc >> 1) ^ mp) & 1) __builtin_amdgcn_s_setprio(1); else __builtin_amdgcn_s_setprio(0);
        f32x2 vv[4];
#pragma unroll
        for (int n = 0; n < 2; ++n)
#pragma unroll
            for (int j = 0; j < 2; ++j) {
                constexpr float sc = (TYPE == T_Q) ? 0.32259534218f : 1.f;
                const f32x2 ac = f32x2{acc[mp][n][2 * j], acc[mp][n][2 * j + 1]};
                if constexpr (FT) { const float a = av[mp][0] * sc, bb = bv[mp][0] * sc;
                                    vv[2 * n + j] = __builtin_elementwise_fma(f32x2{a, a}, ac, f32x2{bb, bb} * f32x2{cf[n][2 * j], cf[n][2 * j + 1]}); }
                else vv[2 * n + j] = __builtin_elementwise_fma(f32x2{av[mp][2 * j], av[mp][2 * j + 1]}, ac, f32x2{bv[mp][2 * j], bv[mp][2 * j + 1]} * f32x2{cf[n][0], cf[n][0]});
            }
        if constexpr (TYPE == T_Z1 || TYPE == T_Z2) { SBAR(); gelu_n<4>(vv); }
        const unsigned pa0 = pk2(vv[0][0], vv[0][1]), pa1 = pk2(vv[1][0], vv[1][1]), pb0 = pk2(vv[2][0], vv[2][1]), pb1 = pk2(vv[3][0], vv[3][1]);
        unsigned t00 = wr ? pb0 : pa0, t01 = wr ? pb1 : pa1, t10 = wr ? pa0 : pb0, t11 = wr ? pa1 : pb1;
        if constexpr (TYPE != T_K && TYPE != T_V) {
            auto x0 = __builtin_amdgcn_permlane16_swap(t00, t10, false, false);
            auto x1 = __builtin_amdgcn_permlane16_swap(t01, t11, false, false);
            t00 = x0[0]; t01 = x1[0]; t10 = x0[1]; t11 = x1[1];
        }
        asm volatile("" : "+v"(t00), "+v"(t01), "+v"(t10), "+v"(t11));
        if (tb < S_) {
            if constexpr (TYPE == T_K) {
                const int t = tb + r15;
                const unsigned kt = (unsigned)OFF_KF + (t >> 5) * KT_BYTES + (t & 31) * 16;
                st64(rws, kt + (g >> 1) * 512 + (g & 1) * 8, u32x2{t00, t01});
                if (g == 1) st64(rws, kt + 1024, u32x2{t10, t11});
                if (g == 2) st64(rws, kt + 1024 + 8, u32x2{0x00003F80u, 0u});
            } else if constexpr (TYPE == T_V) {
                const unsigned vt = (unsigned)OFF_VF + (tb >> 5) * VT_BYTES + ((((tb >> 4) & 1) * 2 + (g & 1)) * V_SLOTS) * 16 + (g >> 1) * 8;
                st64(rws, vt + r15 * 16, u32x2{t00, t01});
                if (r15 >= 4 && r15 < 8) st64(rws, vt + (12 + r15) * 16, u32x2{t10, t11});
                if (r15 == 8) st64(rws, vt + 20 * 16, u32x2{0x3F803F80u, 0x3F803F80u});
                if (r15 == 9) st64(rws, vt + 21 * 16, u32x2{0u, 0u});
            } else {
                unsigned dst;
                if constexpr (TYPE == T_Z2) dst = (unsigned)OFF_Z2R + (unsigned)(((tb >> 3) + (g >> 1)) * FH_ + col0 + 16 * (g & 1) + r15) * 16;
                else dst = (unsigned)(TYPE == T_Q ? OFF_Q : OFF_Z1) + (unsigned)((tb + r15) * (TYPE == T_Q ? 1280 : 1728) + (col0 + 16 * (g & 1) + 8 * (g >> 1)) * 2);
                st128(rws, dst, u32x4{t00, t01, t10, t11});
            }
        }
    }
}

__global__ __launch_bounds__(768, 3) void k1_proj(Params P) {
    extern __shared__ __attribute__((aligned(16))) char smem[];
    const int tid = threadIdx.x, lane = tid & 63, w = __builtin_amdgcn_readfirstlane(tid >> 6);
    STAMP_DECL; STAMP(0); RSTAMP(30);
    const int b = blockIdx.x, j0 = b >> 3, n0 = (b & 7) * 31 + j0;
    if (j0 == 31 || n0 >= 247) {
        if (b == 255 && tid < 64) ((u32x4*)(P.ws + OFF_FLAG))[tid * 8] = u32x4{0u, 0u, 0u, 0u};
        return;
    }
    int rb, cb;
    if (n0 < 133) { cb = n0 / 7; rb = n0 - cb * 7; } else { const int m = n0 - 133; cb = m / 6; rb = 7 + m - cb * 6; }
    const int row0 = rb * BM;
    lds_ptr lds = (lds_ptr)smem;
    const unsigned lds0 = (unsigned)(uintptr_t)smem;
    if (w >= 8) {
        const int L = w - 8;
        Ld ld;
        ld.ra = mk_rsrc(P.x, S_ * 1280);
        const int r8 = lane >> 3, c8 = lane & 7, k4 = lane >> 4, c16 = lane & 15;
#pragma unroll
        for (int i = 0; i < 2; ++i) ld.va[i] = r8 * 1280 + ((c8 ^ ((4 * ((L + i) & 1) + k4) & 7)) << 4);
        ld.sa = (row0 + 40 * L) * 1280;
        ld.da = lds0 + L_A + 5 * L * 1024; ld.db = lds0 + L_B + 4 * L * 1024;
        const unsigned ldw = cb < 5 ? 1280u : 3456u;
        ld.rb = cb < 5 ? mk_rsrc(P.wq, D_ * 1280) : mk_rsrc(P.win, D_ * 3456);
#pragma unroll
        for (int i = 0; i < 2; ++i) ld.vb[i] = k4 * ldw + ((c16 ^ (k4 << 1) ^ (i << 3)) << 4);
        ld.sb = (cb < 5 ? cb : cb - 5) * 256 + 16 * L * ldw; ld.sbinc = 64 * ldw;
        ld.rot = 0;
        ld.rp = mk_rsrc(P.wsp, S_ * 4096); ld.vp = (lane >> 5) * 4096 + (lane & 31) * 128; ld.sp = (unsigned)(b * 8 + L * 2) * 4096; ld.tp = 0;
        STAMP(1);
        if (cb != 18) loader<false>(ld STAMP_PASS);
        else {
#pragma unroll
            for (int j = 0; j < 4; ++j) {
                const int k = 16 * L + 4 * j + k4, c = c16 ^ (k4 << 1) ^ (((j >> 1) & 1) << 3), cc = c & 3;
                if (c < 8) { ld.pk[j] = (const char*)P.win + k * 3456 + 13 * 256 + c * 16; ld.pinc[j] = 64 * 3456; }
                else { ld.pk[j] = (const char*)(c < 12 ? P.wk : P.wv) + k * 40 + (cc == 1 ? 16 : cc == 2 ? 24 : 0); ld.pinc[j] = 64 * 40; }
            }
            loader<true>(ld STAMP_PASS);
        }
        return;
    }
    unsigned tch = 0;
    {
        const int wg = n0;
        if (w == 0) touch_line(tch, mk_rsrc(P.x, S_ * 1280), (unsigned)(wg * 83 + lane) * 128, 0);
        else if (w == 1) touch_line(tch, mk_rsrc(P.x, S_ * 1280), (unsigned)(wg * 83 + min(64 + lane, 82)) * 128, 0);
        else if (w == 2) touch_line(tch, mk_rsrc(P.win, D_ * 3456), (unsigned)(wg * 70 + lane) * 128, 0);
        else if (w == 3) touch_line(tch, mk_rsrc(P.win, D_ * 3456), (unsigned)(wg * 70 + min(64 + lane, 69)) * 128, 0);
        else if (w == 4) touch_line(tch, mk_rsrc(P.wq, D_ * 1280), (unsigned)(wg * 26 + min(lane, 25)) * 128, 0);
    }
    const int wr = w & 1, wc = w >> 1;
    int type, col0;
    if (cb < 5) { type = T_Q; col0 = cb * 128 + wc * 32; }
    else { const int zc = (cb - 5) * 128 + wc * 32;
           if (cb == 18 && wc >= 2) { type = (wc == 2) ? T_K : T_V; col0 = 0; }
           else if (zc < FH_) { type = T_Z1; col0 = zc; } else { type = T_Z2; col0 = zc - FH_; } }
    const bool ft = (type == T_Q || type == T_K || type == T_Z1);
    f32x4 acc[5][2];
#pragma unroll
    for (int m = 0; m < 5; ++m) { acc[m][0] = f32x4{0, 0, 0, 0}; acc[m][1] = f32x4{0, 0, 0, 0}; }
    Stat st; st.s0 = st.q0 = st.s1 = st.q1 = st.cs = f32x4{0, 0, 0, 0};
    STAMP(2);
    if (ft) mainloop<true>(lds, lane, wr, wc, acc, st STAMP_PASS);
    else mainloop<false>(lds, lane, wr, wc, acc, st STAMP_PASS);
    WAIT_VM(0); TOUCH_DONE(tch);
    STAMP(13);
    const int r15 = lane & 15, g = lane >> 4;
    LDSP float* rsum = (LDSP float*)(lds + L_ST); LDSP float* rsq = rsum + 160; LDSP float* csum = rsum + 320; LDSP float* ra = rsum + 448; LDSP float* rbv = rsum + 608;
    LDSP float* p4s = rsum + 768; LDSP float* p4q = rsum + 896;
    if (r15 == 0) { *(LDSP f32x4*)(rsum + 80 * wr + 16 * wc + 4 * g) = st.s0; *(LDSP f32x4*)(p4s + wc * 32 + wr * 16 + 4 * g) = st.s1; }
    if ((r15 >> 2) == g) {
        const int j = r15 & 3;
        rsq[80 * wr + 16 * wc + r15] = j == 0 ? st.q0[0] : j == 1 ? st.q0[1] : j == 2 ? st.q0[2] : st.q0[3];
        p4q[wc * 32 + wr * 16 + r15] = j == 0 ? st.q1[0] : j == 1 ? st.q1[1] : j == 2 ? st.q1[2] : st.q1[3];
    }
    if (g == 0) csum[32 * wc + 16 * wr + r15] = st.cs[0];
    STAMP(19);
    BAR_LDS();
    STAMP(20);
    if (tid < BM) {
        const int tr = tid >= 80, tl = tid - 80 * tr, o = tr * 16 + (tl & 15);
        float sm = rsum[tid], sq = rsq[tid];
        const float s4 = p4s[o] + p4s[32 + o] + p4s[64 + o] + p4s[96 + o], q4 = p4q[o] + p4q[32 + o] + p4q[64 + o] + p4q[96 + o];
        if (tl >= 64) { sm = s4; sq = q4; }
        const float mu = sm * (1.f / D_), var = fmaxf(sq * (1.f / D_) - mu * mu, 0.f);
        const float rs = __builtin_amdgcn_rsqf(var + 1e-5f);
        ra[tid] = rs; rbv[tid] = -mu * rs;
    }
    BAR_LDS();
    STAMP(14);
    const int tw = row0 + 80 * wr;
    switch (type) {
        case T_Q: epilogue<T_Q>(P.ws, acc, csum, ra, rbv, tw, col0, lane, wr, wc STAMP_PASS); break;
        case T_K: epilogue<T_K>(P.ws, acc, csum, ra, rbv, tw, col0, lane, wr, wc STAMP_PASS); break;
        case T_V: epilogue<T_V>(P.ws, acc, csum, ra, rbv, tw, col0, lane, wr, wc STAMP_PASS); break;
        case T_Z1: epilogue<T_Z1>(P.ws, acc, csum, ra, rbv, tw, col0, lane, wr, wc STAMP_PASS); break;
        default: epilogue<T_Z2>(P.ws, acc, csum, ra, rbv, tw, col0, lane, wr, wc STAMP_PASS); break;
    }
    STAMP(15); RSTAMP(31); STAMP_FLUSH_K(0);
}
}
namespace k2 {
constexpr int KV_HALF = 12288, KV_STAGE = 2 * KV_HALF, W_STAGE = 32768;
constexpr int L_KV = 0, L_W = 3 * KV_STAGE, L_RED = L_W + 2 * W_STAGE, L_DUMMY = L_RED + 1024, L_OUT = L_DUMMY + 6144, LDS_BYTES = L_OUT + 8 * 1280;
enum { C_KV0 = 64, C_KV12, C_GAT, C_NORM, C_PUB, C_FLAG, C_ATT = 72  , C_PAIR = 80, C_PROG = 88  , C_UST = 70 };
DI void lds_add(lds_ptr lds, int word) {
    asm volatile("s_mov_b64 exec, 1\n\tds_add_u32 %0, %1\n\ts_mov_b64 exec, -1" :: "v"((unsigned)(L_RED + 4 * word) + (unsigned)(uintptr_t)lds), "v"(1u) : "memory");
}
DI void lds_wait_ge(lds_ptr lds, int word, unsigned target) {
    const volatile LDSP unsigned* p = (const volatile LDSP unsigned*)(lds + L_RED + 4 * word);
    for (unsigned spin = 0; spin < (1u << 26); ++spin) {
        if ((unsigned)__builtin_amdgcn_readfirstlane(*p) >= target) break;
        __builtin_amdgcn_s_sleep(1);
    }
    asm volatile("" ::: "memory");
}
constexpr float M_SHIFT = 100.f;
constexpr int ZSTEP = 2 * FH_ * 16;
struct Params { const u16* wsp; const u16* bsp; const u16* wo; const u16* wout; const u16* x; char* ws; };
#define USE(x) asm volatile("" :: "v"(x))
#define EXP2(x) __builtin_amdgcn_exp2f(x)
DI float lanemax16(const f32x16& s) {
    float a = fmaxf(fmaxf(s[0], s[1]), s[2]), b = fmaxf(fmaxf(s[3], s[4]), s[5]);
#pragma unroll
    for (int r = 6; r < 14; r += 4) { a = fmaxf(fmaxf(a, s[r]), s[r + 1]); b = fmaxf(fmaxf(b, s[r + 2]), s[r + 3]); }
    a = fmaxf(fmaxf(a, s[14]), s[15]);
    return fmaxf(a, b);
}
DI float halves_max(float a) {
    auto rr = __builtin_amdgcn_permlane32_swap(__float_as_uint(a), __float_as_uint(a), false, false);
    return fmaxf(__uint_as_float(rr[0]), __uint_as_float(rr[1]));
}
DI float rowmax32(const f32x16& s) { return halves_max(lanemax16(s)); }
DI void pack16(const f32x16& s, u32x4& p0, u32x4& p1) {
    p0 = u32x4{pk2(s[0], s[1]), pk2(s[2], s[3]), pk2(s[4], s[5]), pk2(s[6], s[7])};
    p1 = u32x4{pk2(s[8], s[9]), pk2(s[10], s[11]), pk2(s[12], s[13]), pk2(s[14], s[15])};
}
DI f32x16 attn_fallback(const char* ws, u32x4 q0, u32x2 q1, int lane) {
    const int r = lane & 31, h = lane >> 5;
    if (h) q1 = u32x2{0u, 0u};
    f32x16 o = {}; float m = -INFINITY;
#pragma unroll 1
    for (int T = 0; T < 64; ++T) {
        const char* kt = ws + OFF_KF + T * KT_BYTES; const char* vt = ws + OFF_VF + T * VT_BYTES;
        const u32x4 k0 = *(const u32x4*)(kt + h * 512 + r * 16); const u32x2 k1 = *(const u32x2*)(kt + 1024 + r * 16 + h * 8);
        const int rv = min(r, 21);
        const u32x4 v0 = *(const u32x4*)(vt + (h * V_SLOTS + rv) * 16), v1 = *(const u32x4*)(vt + ((2 + h) * V_SLOTS + rv) * 16);
        f32x16 s = mfma32(k0, q0, f32x16{}); s = mfma32k8(k1, q1, s);
        const float mn = fmaxf(m, rowmax32(s));
        const float al = (mn == -INFINITY) ? 1.f : EXP2(m - mn);
        const float ms = (mn == -INFINITY) ? 0.f : mn;
#pragma unroll
        for (int i = 0; i < 16; ++i) { s[i] = EXP2(s[i] - ms); o[i] *= al; }
        m = mn;
        u32x4 p0, p1; pack16(s, p0, p1);
        o = mfma32(v0, p0, o); o = mfma32(v1, p1, o);
    }
    return o;
}

struct St {
    u32x4 qa0, qb0; u32x2 qa1, qb1;
    f32x16 sa, sb, oa, ob;
    u32x4 pa0, pa1, pb0, pb1;
    u32x4 k0[2]; u32x2 k1[2]; u32x4 v0[2], v1[2];
};
#define K2_SCHED 4
#define E_(S, i) S[i] = EXP2(S[i]); USE(S[i])
#define C_(P, j, S, i) P[j] = pk2(S[i], S[i + 1]); USE(P[j])
#define K2_STEP(S, P0, P1, SP, PP1, M0, M1, M2, M3, R0, R1, R2)                                          \
    do {                                                                                                 \
        C_(PP1, 3, SP, 14); M0; R0; SBAR(); E_(S, 0); E_(S, 1); E_(S, 2); E_(S, 3); SBAR();              \
        M1; R1; C_(P0, 0, S, 0); E_(S, 4); E_(S, 5); C_(P0, 1, S, 2); E_(S, 6); E_(S, 7); SBAR();        \
        M2; R2; C_(P0, 2, S, 4); E_(S, 8); E_(S, 9); C_(P0, 3, S, 6); E_(S, 10); E_(S, 11); SBAR();      \
        M3; C_(P1, 0, S, 8); E_(S, 12); E_(S, 13); C_(P1, 1, S, 10); E_(S, 14); E_(S, 15); C_(P1, 2, S, 12); SBAR(); \
    } while (0)
#define K2_FLIP 1
#define K2_PLO 1
#define K2_PHI 2
#define K2_ALIGN 1
template <int STEP> DI void prio_step() {
    if constexpr (STEP == 0) __builtin_amdgcn_s_setprio(3);
    if constexpr (STEP == 3) __builtin_amdgcn_s_setprio(2);
    if constexpr (STEP == 6) __builtin_amdgcn_s_setprio(1);
}
struct Pr { volatile LDSP unsigned* mine; const volatile LDSP unsigned* other; unsigned it, seen; };
DI void prio_rel(Pr& p) {
}
template <int I>
DI void att_iter(St& s, Pr& pr, lds_ptr pk0, lds_ptr pk1, lds_ptr pv) {
    constexpr int c = I & 1, n = c ^ 1;
    prio_rel(pr);
    prio_step<2 * I>();
    K2_STEP(s.sa, s.pa0, s.pa1, s.sb, s.pb1,
            s.sb = mfma32(s.k0[c], s.qb0, f32x16{}),
            s.sb = mfma32k8(s.k1[c], s.qb1, s.sb),
            s.ob = mfma32(s.v0[n], s.pb0, s.ob),
            s.ob = mfma32(s.v1[n], s.pb1, s.ob),
            { s.k0[n] = ldsr128(pk0 + I * KT_BYTES); s.k1[n] = *(const LDSP u32x2*)(pk1 + I * KT_BYTES); },
            { s.v0[c] = ldsr128(pv + I * VT_BYTES); },
            { s.v1[c] = ldsr128(pv + I * VT_BYTES + 2 * V_SLOTS * 16); });
    prio_step<2 * I + 1>();
    K2_STEP(s.sb, s.pb0, s.pb1, s.sa, s.pa1,
            s.sa = mfma32(s.k0[n], s.qa0, f32x16{}),
            s.sa = mfma32k8(s.k1[n], s.qa1, s.sa),
            s.oa = mfma32(s.v0[c], s.pa0, s.oa),
            s.oa = mfma32(s.v1[c], s.pa1, s.oa),
            {}, {}, {});
}
DI void att_loop(St& s, lds_ptr lds, lds_ptr kvb, unsigned k0o, unsigned k1o, unsigned vo, int w STAMP_ARG) {
    int buf = 0;
    Pr pr; pr.mine = (volatile LDSP unsigned*)(lds + L_RED) + C_PROG + w; pr.other = (const volatile LDSP unsigned*)(lds + L_RED) + C_PROG + (w ^ 4); pr.it = 0; pr.seen = 0;
    const volatile LDSP unsigned* cw = (const volatile LDSP unsigned*)(lds + L_RED);
    unsigned seen = 0;
#pragma unroll 1
    for (int ss = 0; ss < 8; ++ss) {
        const int word = ss == 0 ? C_KV0 : ss == 1 ? C_KV12 : C_GAT;
        const unsigned target = ss == 1 ? 8u : ss < 3 ? 0u : 4u * (ss - 1);
        if ((unsigned)__builtin_amdgcn_readfirstlane(seen) < target) lds_wait_ge(lds, word, target);
        if (ss > 0) lds_wait_ge(lds, C_ATT + ss - 1, 8u);
        lds_ptr kv = kvb + buf * KV_STAGE;
        att_iter<0>(s, pr, kv + k0o, kv + k1o, kv + vo);
        if (ss == 0) { WAIT_VM(0); lds_add(lds, C_KV12); }
        att_iter<1>(s, pr, kv + k0o, kv + k1o, kv + vo);
        att_iter<2>(s, pr, kv + k0o, kv + k1o, kv + vo);
        seen = cw[ss == 0 ? C_KV12 : C_GAT];
        att_iter<3>(s, pr, kv + k0o, kv + k1o, kv + vo);
        asm volatile("s_waitcnt lgkmcnt(0)" ::: "memory");
        lds_add(lds, C_ATT + ss);
        buf = (buf == 2) ? 0 : buf + 1;
    }
}
DI void attention(const Params& P, lds_ptr lds, unsigned lds0, int b, int w, int lane STAMP_ARG) {
    const int p = w & 3, hf = w >> 2, r31 = lane & 31, h = lane >> 5;
    char* ws = P.ws;
    St s;
    __builtin_amdgcn_s_setprio(2);
    BAR_LDS();
    const rsrc_t rkv = mk_rsrc(ws, (unsigned)OFF_Z1);
    unsigned skv[3], kinc[3];
#pragma unroll
    for (int i = 0; i < 3; ++i) {
        const int pc = 3 * w + i, ph = pc >= 12, j = pc - ph * 12;
        skv[i] = j < 6 ? (unsigned)OFF_KF + (ph * 32 + 1) * KT_BYTES + j * 1024 : (unsigned)OFF_VF + (ph * 32) * VT_BYTES + (j - 6) * 1024;
        kinc[i] = j < 6 ? 4 * KT_BYTES : 4 * VT_BYTES;
    }
    const unsigned kvdst = lds0 + L_KV + 3 * w * 1024;
    STAMP(18);
#pragma unroll
    for (int i = 0; i < 3; ++i) bdma(rkv, lane * 16, skv[i], kvdst + i * 1024);
#pragma unroll
    for (int i = 0; i < 2; ++i) {
        const int pc = min(2 * w + i, 13);
        const unsigned src = pc < 10 ? (unsigned)OFF_Q + (8 * b) * 1280 + pc * 1024 : (unsigned)OFF_KF + (pc >= 12 ? 32 * KT_BYTES : 0) + (pc & 1) * 1024;
        bdma(rkv, lane * 16, src, lds0 + L_W + W_STAGE + pc * 1024);
    }
    STAMP(19);
    WAIT_VM(0);
    lds_add(lds, C_KV0);
#pragma unroll
    for (int st = 1; st < 3; ++st)
#pragma unroll
        for (int i = 0; i < 3; ++i) bdma(rkv, lane * 16, skv[i] + st * kinc[i], kvdst + st * KV_STAGE + i * 1024);
    STAMP(20);
    lds_wait_ge(lds, C_KV0, 8u);
    STAMP(21);
    const int tA = 8 * b + 2 * p;
    {
        lds_ptr qp = lds + L_W + W_STAGE + (2 * p) * 1280 + r31 * 40;
        auto rd16 = [&](lds_ptr a) { const u32x2 lo = *(const LDSP u32x2*)a, hi = *(const LDSP u32x2*)(a + 8); return u32x4{lo[0], lo[1], hi[0], hi[1]}; };
        s.qa0 = rd16(qp + h * 16); s.qb0 = rd16(qp + 1280 + h * 16);
        s.qa1 = *(const LDSP u32x2*)(qp + 32); s.qb1 = *(const LDSP u32x2*)(qp + 1280 + 32);
        lds_ptr k0p = lds + L_W + W_STAGE + 10240;
        const u32x4 k00 = ldsr128(k0p + h * 512 + r31 * 16); const u32x2 k01 = *(const LDSP u32x2*)(k0p + 1024 + r31 * 16 + h * 8);
        lds_ptr kfp = k0p + hf * 2048;
        s.k0[0] = ldsr128(kfp + h * 512 + r31 * 16); s.k1[0] = *(const LDSP u32x2*)(kfp + 1024 + r31 * 16 + h * 8);
        if (h) { s.qa1 = u32x2{0u, 0u}; s.qb1 = u32x2{0u, 0u}; }
        f32x16 a = mfma32(k00, s.qa0, f32x16{}), c = mfma32(k00, s.qb0, f32x16{});
        a = mfma32k8(k01, s.qa1, a); c = mfma32k8(k01, s.qb1, c);
        float ma = lanemax16(a), mb = lanemax16(c);
#pragma unroll
        for (int i = 0; i < 3; ++i) {
            lds_ptr kp = lds + L_KV + i * KT_BYTES;
            const u32x4 kx0 = ldsr128(kp + h * 512 + r31 * 16); const u32x2 kx1 = *(const LDSP u32x2*)(kp + 1024 + r31 * 16 + h * 8);
            a = mfma32(kx0, s.qa0, f32x16{}); c = mfma32(kx0, s.qb0, f32x16{});
            a = mfma32k8(kx1, s.qa1, a); c = mfma32k8(kx1, s.qb1, c);
            ma = fmaxf(ma, lanemax16(a)); mb = fmaxf(mb, lanemax16(c));
        }
        ma = halves_max(ma) + M_SHIFT; mb = halves_max(mb) + M_SHIFT;
        if (h) { s.qa1[0] = pk2(-ma, 0.f); s.qb1[0] = pk2(-mb, 0.f); }
    }
    s.sa = mfma32(s.k0[0], s.qa0, f32x16{}); s.sa = mfma32k8(s.k1[0], s.qa1, s.sa);
    s.oa = f32x16{}; s.ob = f32x16{}; s.sb = f32x16{};
    s.pb0 = u32x4{0, 0, 0, 0}; s.pb1 = u32x4{0, 0, 0, 0}; s.v0[1] = u32x4{0, 0, 0, 0}; s.v1[1] = u32x4{0, 0, 0, 0};
    lds_ptr kvb = lds + L_KV + hf * KV_HALF;
    const unsigned k0o = h * 512 + r31 * 16, k1o = 1024 + r31 * 16 + h * 8, vo = 6144 + (h * V_SLOTS + min(r31, 21)) * 16;
    STAMP(3);
    att_loop(s, lds, kvb, k0o, k1o, vo, w STAMP_PASS);
    STAMP(15);
    s.pb1[3] = pk2(s.sb[14], s.sb[15]);
    s.ob = mfma32(s.v0[1], s.pb0, s.ob); s.ob = mfma32(s.v1[1], s.pb1, s.ob);
    __builtin_amdgcn_s_setprio(3);
    lds_wait_ge(lds, C_ATT + 5, 8u);
    LDSP f32x4* xs = (LDSP f32x4*)(lds + L_KV + 2 * KV_STAGE) + w * 3 * 64;
    LDSP f32x4* xr = (LDSP f32x4*)(lds + L_KV + 2 * KV_STAGE) + (w ^ 4) * 3 * 64;
    if (hf == 0) {
#pragma unroll
        for (int i = 0; i < 3; ++i) xs[i * 64 + lane] = f32x4{s.ob[4 * i], s.ob[4 * i + 1], s.ob[4 * i + 2], s.ob[4 * i + 3]};
    } else {
#pragma unroll
        for (int i = 0; i < 3; ++i) xs[i * 64 + lane] = f32x4{s.oa[4 * i], s.oa[4 * i + 1], s.oa[4 * i + 2], s.oa[4 * i + 3]};
        s.oa = s.ob; s.qa0 = s.qb0; s.qa1 = s.qb1;
    }
    asm volatile("s_waitcnt lgkmcnt(0)" ::: "memory");
    lds_add(lds, C_PAIR + w);
    lds_wait_ge(lds, C_PAIR + (w ^ 4), 1u);
    STAMP(16);
    f32x16 o = s.oa;
#pragma unroll
    for (int i = 0; i < 3; ++i) { const f32x4 t = xr[i * 64 + lane]; o[4 * i] += t[0]; o[4 * i + 1] += t[1]; o[4 * i + 2] += t[2]; o[4 * i + 3] += t[3]; }
    auto lr = __builtin_amdgcn_permlane32_swap(__float_as_uint(o[8]), __float_as_uint(o[8]), false, false);
    float l = __uint_as_float(lr[1]);
    bool ok = (l >= 0x1p-110f) && (l <= 0x1p120f);
#pragma unroll
    for (int i = 0; i < 12; ++i) ok = ok && (fabsf(o[i]) <= 0x1p126f || (h && i >= 8));
    if (__builtin_expect(!__all(ok), 0)) {
        o = attn_fallback(ws, s.qa0, s.qa1, lane);
        lr = __builtin_amdgcn_permlane32_swap(__float_as_uint(o[8]), __float_as_uint(o[8]), false, false);
        l = __uint_as_float(lr[1]);
    }
    const float rl = 1.f / l;
    lds_ptr ob = lds + L_OUT + w * 1280;
    *(LDSP u32x2*)(ob + r31 * 40 + h * 8) = u32x2{pk2(o[0] * rl, o[1] * rl), pk2(o[2] * rl, o[3] * rl)};
    *(LDSP u32x2*)(ob + r31 * 40 + h * 8 + 16) = u32x2{pk2(o[4] * rl, o[5] * rl), pk2(o[6] * rl, o[7] * rl)};
    if (!h) *(LDSP u32x2*)(ob + r31 * 40 + 32) = u32x2{pk2(o[8] * rl, o[9] * rl), pk2(o[10] * rl, o[11] * rl)};
    asm volatile("s_waitcnt lgkmcnt(0)" ::: "memory");
    const rsrc_t rou = mk_rsrc(ws + OFF_OU, S_ * 3072);
    st128(rou, (unsigned)((tA + hf) * 3072 + lane * 16), ldsr128(ob + lane * 16));
    if (lane < 16) st128(rou, (unsigned)((tA + hf) * 3072 + 1024 + lane * 16), ldsr128(ob + 1024 + lane * 16));
}

DI void zload(u32x4& z, rsrc_t r, unsigned voff, unsigned soff) {
    asm volatile("buffer_load_dwordx4 %0, %1, %2, %3 offen sc1" : "+v"(z) : "v"(voff), "s"(r), "s"(soff) : "memory");
}
#define K2_GSPLIT 1
#define K2_PFSS 4
#define K2_POLL 16
#define GBURST_PRIO 2
#define GSTEP_PRIO 3
struct Gs {
    rsrc_t rkv, rw, rz; unsigned vkv, vw[8], skv[6], sw, wstr, dkv, dw, zoff;
    u32x4 z[16]; f32x16 g0, g1; u32x4 wa0, wa1;
};
template <int I> DI void kv_dma(const Gs& g) { bdma(g.rkv, g.vkv, g.skv[I], g.dkv + I * 1024); }
template <int I> DI void w_dma(const Gs& g) { bdma(g.rw, g.vw[I], g.sw + I * g.wstr, g.dw + I * 1024); }
DI void z_loads(Gs& g, unsigned zs) {
#pragma unroll
    for (int i = 0; i < 16; ++i) zload(g.z[i], g.rz, g.zoff, zs + i * ZSTEP);
}
template <int I> DI void kv_all(const Gs& g) { kv_dma<I>(g); if constexpr (I < 5) kv_all<I + 1>(g); }
template <int I> DI void w_all(const Gs& g) { w_dma<I>(g); if constexpr (I < 7) w_all<I + 1>(g); }
#define Z_TIE(g) "+v"(g.z[0]), "+v"(g.z[1]), "+v"(g.z[2]), "+v"(g.z[3]), "+v"(g.z[4]), "+v"(g.z[5]), "+v"(g.z[6]), "+v"(g.z[7]), \
                 "+v"(g.z[8]), "+v"(g.z[9]), "+v"(g.z[10]), "+v"(g.z[11]), "+v"(g.z[12]), "+v"(g.z[13]), "+v"(g.z[14]), "+v"(g.z[15])
template <int I> DI void g_burst(Gs& g, lds_ptr wp, unsigned wx) {
    u32x4 wn0, wn1;
    if constexpr (I == 0) { g.wa0 = ldsr128(wp + wx); g.wa1 = ldsr128(wp + wx + 16384); }
    if constexpr (I < 15) { wn0 = ldsr128(wp + (wx ^ ((I + 1) * 32))); wn1 = ldsr128(wp + (wx ^ ((I + 1) * 32)) + 16384); }
    g.g0 = mfma32(g.z[I], g.wa0, g.g0); g.g1 = mfma32(g.z[I], g.wa1, g.g1);
    if constexpr (I < 15) { g.wa0 = wn0; g.wa1 = wn1; }
    SBAR();
    if constexpr (I < 15) g_burst<I + 1>(g, wp, wx);
}
template <int J> DI void dma_piece(const Gs& g) { if constexpr (J < 8) w_dma<J>(g); else kv_dma<J - 8>(g); }
template <int N> DI void z_wait(u32x4& z) { asm volatile("s_waitcnt vmcnt(%1)" : "+v"(z) : "i"(N) : "memory"); }
template <int I, bool FIRST> DI void g_step(Gs& g, lds_ptr wp, unsigned wx, unsigned zs) {
    u32x4 wn0, wn1;
    if constexpr (I == 0) { g.wa0 = ldsr128(wp + wx); g.wa1 = ldsr128(wp + wx + 16384); }
    if constexpr (I < 15) { wn0 = ldsr128(wp + (wx ^ ((I + 1) * 32))); wn1 = ldsr128(wp + (wx ^ ((I + 1) * 32)) + 16384); }
    constexpr int nd = I < 3 ? 2 : I < 11 ? 1 : 0;
    constexpr int before = I <= 3 ? 2 * I : I <= 11 ? I + 3 : 14;
    z_wait<FIRST ? 15 + before : 29 - nd>(g.z[I]);
    g.g0 = mfma32(g.z[I], g.wa0, g.g0);
    SBAR();
    if constexpr (I < 3) { dma_piece<2 * I>(g); dma_piece<2 * I + 1>(g); } else if constexpr (I < 11) dma_piece<I + 3>(g);
    else __builtin_amdgcn_s_sleep(1);
    SBAR();
    g.g1 = mfma32(g.z[I], g.wa1, g.g1);
    zload(g.z[I], g.rz, g.zoff, zs + I * ZSTEP);
    if constexpr (I < 15) { g.wa0 = wn0; g.wa1 = wn1; }
    SBAR();
    if constexpr (I < 15) g_step<I + 1, FIRST>(g, wp, wx, zs);
}
template <bool HASG>
DI void gating(const Params& P, lds_ptr lds, unsigned lds0, int b, int gw, int lane, int t0, int f0 STAMP_ARG) {
    char* ws = P.ws;
    const int t256 = gw * 64 + lane, r31 = lane & 31, h = lane >> 5;
    __builtin_amdgcn_s_setprio(3);
    const char* zr0 = ws + OFF_Z2R + (unsigned)(b * FH_ * 16);
    u32x4 c[4];
#pragma unroll
    for (int i = 0; i < 4; ++i) c[i] = *(const u32x4*)(zr0 + (i < 3 || t256 < FH_ - 768 ? t256 + 256 * i : t256) * 16);
    if (gw == 0 && lane < 32) ((LDSP unsigned*)(lds + L_RED))[64 + lane] = 0u;
    BAR_LDS();
    STAMP(21);
    USE(c[0][0]); USE(c[1][0]); USE(c[2][0]); USE(c[3][0]);
    STAMP(22);
    if (t256 >= FH_ - 768) c[3] = u32x4{0, 0, 0, 0};
    LDSP float* red = (LDSP float*)(lds + L_RED);
    {
        float v[16];
#pragma unroll
        for (int j = 0; j < 8; ++j) { v[j] = 0.f; v[8 + j] = 0.f; }
#pragma unroll
        for (int i = 0; i < 4; ++i)
#pragma unroll
            for (int j = 0; j < 4; ++j) {
                const float lo = bflo(c[i][j]), hi = bfhi(c[i][j]);
                v[2 * j] += lo; v[2 * j + 1] += hi; v[8 + 2 * j] = __builtin_fmaf(lo, lo, v[8 + 2 * j]); v[8 + 2 * j + 1] = __builtin_fmaf(hi, hi, v[8 + 2 * j + 1]);
            }
        float u[8], t[4];
#pragma unroll
        for (int j = 0; j < 8; ++j) { auto r = __builtin_amdgcn_permlane32_swap(__float_as_uint(v[j]), __float_as_uint(v[j + 8]), false, false); u[j] = __uint_as_float(r[0]) + __uint_as_float(r[1]); }
#pragma unroll
        for (int j = 0; j < 4; ++j) { auto r = __builtin_amdgcn_permlane16_swap(__float_as_uint(u[j]), __float_as_uint(u[j + 4]), false, false); t[j] = __uint_as_float(r[0]) + __uint_as_float(r[1]); }
#pragma unroll
        for (int j = 0; j < 4; ++j) {
            t[j] += __uint_as_float(__builtin_amdgcn_update_dpp(0, __float_as_uint(t[j]), 0x128, 0xf, 0xf, false));
            t[j] += __uint_as_float(__builtin_amdgcn_update_dpp(0, __float_as_uint(t[j]), 0x124, 0xf, 0xf, false));
            t[j] += __uint_as_float(__builtin_amdgcn_update_dpp(0, __float_as_uint(t[j]), 0x122, 0xf, 0xf, false));
            t[j] += __uint_as_float(__builtin_amdgcn_update_dpp(0, __float_as_uint(t[j]), 0x121, 0xf, 0xf, false));
        }
        if ((lane & 15) == 0) { const int rho = lane >> 4; *(LDSP f32x4*)(red + gw * 16 + (rho >> 1) * 8 + (rho & 1) * 4) = f32x4{t[0], t[1], t[2], t[3]}; }
        asm volatile("s_waitcnt lgkmcnt(0)" ::: "memory");
        lds_add(lds, C_NORM);
        STAMP(2);
        lds_wait_ge(lds, C_NORM, 4u);
        float sm = 0, sq = 0;
#pragma unroll
        for (int j = 0; j < 4; ++j) { sm += red[j * 16 + (lane & 7)]; sq += red[j * 16 + 8 + (lane & 7)]; }
        const float mu = sm * (1.f / FH_), var = fmaxf(sq * (1.f / FH_) - mu * mu, 0.f), rs = __builtin_amdgcn_rsqf(var + 1e-5f), sh = -mu * rs;
        float a[8], bb[8];
#pragma unroll
        for (int j = 0; j < 8; ++j) { a[j] = __uint_as_float(__builtin_amdgcn_readlane(__float_as_uint(rs), j)); bb[j] = __uint_as_float(__builtin_amdgcn_readlane(__float_as_uint(sh), j)); }
        const rsrc_t zw = mk_rsrc(ws + OFF_Z2N + (unsigned)(b * FH_ * 16), FH_ * 16);
#pragma unroll
        for (int i = 0; i < 4; ++i) {
            u32x4 o;
#pragma unroll
            for (int j = 0; j < 4; ++j) o[j] = pk2(__builtin_fmaf(bflo(c[i][j]), a[2 * j], bb[2 * j]), __builtin_fmaf(bfhi(c[i][j]), a[2 * j + 1], bb[2 * j + 1]));
            __builtin_amdgcn_raw_buffer_store_b128(o, zw, (t256 + 256 * i) * 16, 0, 16);
        }
    }
    STAMP(23);
    Gs g;
    g.rkv = mk_rsrc(ws + OFF_KF, (unsigned)(OFF_Z1 - OFF_KF)); g.rw = mk_rsrc(P.wsp, S_ * 4096); g.rz = mk_rsrc(ws + OFF_Z2N, (unsigned)(S_ * FH_ * 2 + 4096));
    g.vkv = lane * 16;
#pragma unroll
    for (int i = 0; i < 8; ++i) g.vw[i] = (lane >> 5) * 4096 + (((lane & 31) ^ ((2 * i + (lane >> 5)) & 15)) << 4);
    const int rot = 0;
    const unsigned sw0 = (t0 + 16 * gw) * 4096;
    g.wstr = 2 * 4096;
    g.sw = sw0 + rot * 512;
    const unsigned wdst = lds0 + L_W + 8 * gw * 1024;
    if (HASG) { g.dw = wdst; w_all<0>(g); asm volatile("s_waitcnt vmcnt(8)" ::: "memory"); }
    else WAIT_VM(0);
    unsigned* flags = (unsigned*)(ws + OFF_FLAG);
    if (lane == 0) {
        const unsigned old = __hip_atomic_fetch_add((LDSP unsigned*)(lds + L_RED) + C_PUB, 1u, __ATOMIC_RELAXED, __HIP_MEMORY_SCOPE_WORKGROUP);
        if (old == 3u) __hip_atomic_store(flags + (b >> 2) * 32 + (b & 3), 1u, __ATOMIC_RELAXED, __HIP_MEMORY_SCOPE_AGENT);
    }
    STAMP(5);
    const int ph = gw >> 1;
    const unsigned kvinc = (gw & 1) ? 4 * VT_BYTES : 4 * KT_BYTES;
    {
        const unsigned base = ((gw & 1) ? (unsigned)(OFF_VF - OFF_KF) + (ph * 32) * VT_BYTES : (ph * 32 + 1) * KT_BYTES) + 3 * kvinc;
#pragma unroll
        for (int i = 0; i < 6; ++i) g.skv[i] = base + i * 1024;
    }
    const unsigned kvdst = lds0 + L_KV + 6 * gw * 1024;
    if constexpr (!HASG) {
        lds_add(lds, C_GAT);
        unsigned tw = 0;
        const unsigned vpw = (lane >> 2) * 4096 + (lane & 3) * 128, spw = (unsigned)((b & 7) * 256 + (((b >> 3) - 28) * 4 + gw) * 16) * 4096;
        touch_line(tw, g.rw, vpw, spw + 512);
        int buf = 0;
#pragma unroll 1
        for (int m = 0; m < 6; ++m) {
            lds_wait_ge(lds, C_ATT + m, 8u);
            WAIT_VM(0); TOUCH_DONE(tw);
            touch_line(tw, g.rw, vpw, spw + (m + 2) * 512);
            if (m < 5) {
                g.dkv = kvdst + buf * KV_STAGE;
                kv_all<0>(g);
#pragma unroll
                for (int i = 0; i < 6; ++i) g.skv[i] += kvinc;
            }
            WAIT_VM(0);
            if (m < 5) lds_add(lds, C_GAT);
            buf = (buf == 2) ? 0 : buf + 1;
        }
        TOUCH_DONE(tw);
        lds_wait_ge(lds, C_ATT + K2_PFSS, 8u);
        {
            const int part = ((b >> 3) - 28) * 4 + gw;
            const unsigned colb = (lane % 10) * 128u, rsub = min(lane / 10, 5);
            const rsrc_t rr = part < 7 ? mk_rsrc(P.wo, D_ * 1280) : mk_rsrc(P.wout, FH_ * 1280);
            const unsigned r0 = (unsigned)((part < 7 ? part : part - 7) * 96) * 1280;
            touch_lines8(rr, rsub * 1280 + colb, r0, 6 * 1280);
            touch_lines8(rr, rsub * 1280 + colb, r0 + 48 * 1280, 6 * 1280);
            touch_lines8(mk_rsrc(P.x, S_ * 1280), min(rsub, 1u) * 1280 + colb, (unsigned)((b & 7) * 256 + part * 16) * 1280, 2 * 1280);
        }
        return;
    }
    g.zoff = (h * FH_ + f0 + r31) * 16;
    g.g0 = f32x16{}; g.g1 = f32x16{};
#pragma unroll
    for (int i = 0; i < 16; ++i) g.z[i] = u32x4{0, 0, 0, 0};
    lds_ptr wb = lds + L_W;
    const unsigned wx = r31 * 512 + (((lane & 15) ^ h) << 4);
    if (gw == 0) {
        const u32x4* fp = (const u32x4*)flags + lane * 8;
        for (unsigned spin = 0; spin < (1u << 22); ++spin) {
            __builtin_amdgcn_s_sleep(K2_POLL);
            u32x4 f;
            asm volatile("global_load_dwordx4 %0, %1, off sc1\n\ts_waitcnt vmcnt(0)" : "=&v"(f) : "v"(fp) : "memory");
            if (__all(f[0] == 1u && f[1] == 1u && f[2] == 1u && f[3] == 1u)) break;
        }
        lds_add(lds, C_FLAG);
    } else lds_wait_ge(lds, C_FLAG, 1u);
    STAMP(6);
    WAIT_VM(0);
    lds_add(lds, C_GAT);
    __builtin_amdgcn_s_setprio(GSTEP_PRIO);
    z_loads(g, rot * 16 * ZSTEP);
    int buf = 0;
    auto stage_head = [&](int m) {
        lds_wait_ge(lds, C_GAT, 4u * (m + 1));
        if (m < 5) lds_wait_ge(lds, C_ATT + m, 8u);
        if (m == 5) g.rkv = mk_rsrc(ws, 0);
        g.dkv = (m < 5) ? kvdst + buf * KV_STAGE : lds0 + L_DUMMY; g.dw = wdst + ((m + 1) & 1) * W_STAGE;
        g.sw = sw0 + ((m + 1 + rot) & 7) * 512;
        if (m == 7) {
            g.rz = mk_rsrc(ws, 0);
            g.rw = mk_rsrc(ws + OFF_Z1, S_ * 1728); g.sw = (t0 + 16 * gw) * 1728 + (f0 >> 7) * 256; g.wstr = 4 * 1728;
#pragma unroll
            for (int i = 0; i < 8; ++i) g.vw[i] = i < 4 ? (lane >> 4) * 1728 + (((lane & 15) ^ ((4 * i + (lane >> 4)) & 15)) << 4) : 0x7f000000u;
        }
    };
    auto stage_tail = [&]() {
#pragma unroll
        for (int i = 0; i < 6; ++i) g.skv[i] += kvinc;
        asm volatile("s_waitcnt vmcnt(6) lgkmcnt(0)" ::: "memory");
        lds_add(lds, C_GAT);
        buf = (buf == 2) ? 0 : buf + 1;
    };
    stage_head(0); g_step<0, true>(g, wb, wx, ((1 + rot) & 7) * 16 * ZSTEP); stage_tail();
#pragma unroll 1
    for (int m = 1; m < 8; ++m) { stage_head(m); g_step<0, false>(g, wb + (m & 1) * W_STAGE, wx, ((m + 1 + rot) & 7) * 16 * ZSTEP); stage_tail(); }
    asm volatile("s_waitcnt vmcnt(0)" : Z_TIE(g) :: "memory");
    STAMP(15);
    lds_wait_ge(lds, C_GAT, 36u);
#pragma unroll
    for (int u = 0; u < 2; ++u) {
        const int r = 32 * u + r31;
        const float bias = bf2f(P.bsp[t0 + r]);
        const f32x16& gg = u ? g.g1 : g.g0;
#pragma unroll
        for (int i = 0; i < 4; ++i) {
            const int ch = (4 * gw + i) ^ (r & 15);
            u32x2 o = u32x2{0u, 0u};
            if (f0 < FH_) {
                const u32x2 z1 = *(const LDSP u32x2*)(lds + L_W + (r >> 4) * 8192 + (r & 15) * 256 + ch * 16 + h * 8);
                o = u32x2{pk2(bflo(z1[0]) * (gg[4 * i] + bias), bfhi(z1[0]) * (gg[4 * i + 1] + bias)),
                          pk2(bflo(z1[1]) * (gg[4 * i + 2] + bias), bfhi(z1[1]) * (gg[4 * i + 3] + bias))};
            }
            *(LDSP u32x2*)(lds + L_W + W_STAGE + r * 256 + ch * 16 + h * 8) = o;
        }
    }
    asm volatile("s_waitcnt lgkmcnt(0)" ::: "memory");
    lds_add(lds, C_UST);
    lds_wait_ge(lds, C_UST, 4u);
    const rsrc_t rou = mk_rsrc(ws + OFF_OU, S_ * 3072);
#pragma unroll
    for (int i = 0; i < 4; ++i) {
        const int r = 16 * gw + 4 * i + (lane >> 4), c = lane & 15;
        st128(rou, (unsigned)((t0 + r) * 3072 + 1280 + (f0 >> 7) * 256 + c * 16), ldsr128(lds + L_W + W_STAGE + r * 256 + ((c ^ (r & 15)) << 4)));
    }
}

__global__ __launch_bounds__(768, 3) void k2_mix(Params P) {
    extern __shared__ __attribute__((aligned(16))) char smem[];
    const int lane = threadIdx.x & 63, w = __builtin_amdgcn_readfirstlane(threadIdx.x >> 6);
    STAMP_DECL; STAMP(0); RSTAMP(30);
    const int b = blockIdx.x, j = b >> 3;
    lds_ptr lds = (lds_ptr)smem; const unsigned lds0 = (unsigned)(uintptr_t)smem;
    if (w < 8) attention(P, lds, lds0, b, w, lane STAMP_PASS);
    else if (j < 28) { const int rbg = (b & 7) * 4 + j / 7, cbg = j % 7; gating<true>(P, lds, lds0, b, w - 8, lane, rbg * 64, cbg * 128 + (w - 8) * 32 STAMP_PASS); }
    else gating<false>(P, lds, lds0, b, w - 8, lane, 0, 0 STAMP_PASS);
    STAMP(17); RSTAMP(31); STAMP_FLUSH_K(1);
}
}
namespace k3 {
#define K3_AUX 16
constexpr int BM = 96, A_BYTES = BM * 128, STAGE = 20480, NST = 7, NKS = 24, LDS_BYTES = NST * STAGE;
struct Params { const u16* x; const u16* wo; const u16* wout; const char* ws; u16* out; };
struct Ld { rsrc_t ra, rb0, rb1, rx; unsigned va[2], vb[2], sa, sb, da, db, vx, sx; int rot; };
constexpr int L_X = 3 * STAGE;
template <int S> DI void ld_issue(const Ld& L) {
    constexpr int so = (S % NST) * STAGE;
    int gs = S + L.rot; if (gs >= NKS) gs -= NKS;
    const bool lo = gs < 10;
    const rsrc_t rb = lo ? L.rb0 : L.rb1;
    const unsigned sb = L.sb + (unsigned)(lo ? gs : gs - 10) * (64 * 1280);
#pragma unroll
    for (int j = 0; j < 3; ++j) bdma(L.ra, L.va[j & 1], L.sa + gs * 128 + j * 8 * 3072, L.da + so + j * 1024);
#pragma unroll
    for (int j = 0; j < 2; ++j) bdma(rb, L.vb[j], sb + j * 8 * 1280, L.db + so + j * 1024);
}
constexpr int issued_at(int S) { return (S < 3 ? 2 + 2 * S : S + 5) < NKS ? (S < 3 ? 2 + 2 * S : S + 5) : NKS - 1; }
template <int A, int B> DI void ld_range(const Ld& L) { if constexpr (A <= B) { ld_issue<A>(L); ld_range<A + 1, B>(L); } }
template <int S> DI void ld_step(const Ld& L) {
    if constexpr (S > 0) ld_range<issued_at(S - 1) + 1, issued_at(S)>(L);
    if constexpr (S == 19) {
#pragma unroll
        for (int j = 0; j < 3; ++j) bdma(L.rx, L.vx, L.sx + j * 8 * 1280, L.da + L_X + j * 1024);
    }
    constexpr int young = 5 * (issued_at(S) - (S + 1)) + (S >= 19 && S < NKS - 2 ? 3 : 0);
    asm volatile("s_waitcnt vmcnt(%0) lgkmcnt(0)\n\ts_barrier" :: "i"(young) : "memory");
    if constexpr (S + 2 < NKS) ld_step<S + 1>(L);
}
struct Frag { u32x4 x[3], w[2]; };
struct Ctx { unsigned xo[3], wo0[3], wo1[3]; };
template <int S> DI void rd(Frag& f, lds_ptr lds, const Ctx& c) {
    constexpr int slot = S % NST, grp = slot / 3, so = (slot % 3) * STAGE;
#pragma unroll
    for (int n = 0; n < 2; ++n) {
        lds_ptr wp = lds + (n ? c.wo1[grp] : c.wo0[grp]) + so;
        const u32x2 lo = ldstr(wp), hi = ldstr(wp + 512);
        f.w[n] = u32x4{lo[0], lo[1], hi[0], hi[1]};
    }
#pragma unroll
    for (int m = 0; m < 3; ++m) f.x[m] = ldsr128(lds + c.xo[grp] + so + m * 2048);
}
template <int S> DI void stage(lds_ptr lds, const Ctx& c, Frag& fc, Frag& fn, f32x4 (&acc)[3][2] STAMP_ARG) {
    if constexpr (S + 1 < NKS) {
        BAR_LDS();
        STAMP(2 + S);
        rd<S + 1>(fn, lds, c);
        SBAR();
    }
#pragma unroll
    for (int m = 0; m < 3; ++m)
#pragma unroll
        for (int n = 0; n < 2; ++n) acc[m][n] = mfma16(fc.w[n], fc.x[m], acc[m][n]);
    SBAR();
    if constexpr (S + 1 < NKS) stage<S + 1>(lds, c, fn, fc, acc STAMP_PASS);
}

template <int KH>
DI void finish(const Params& P, lds_ptr lds, const f32x4 (&acc)[3][2], int w, int lane, int wr, int wc, int row0, int rowlim, int cb STAMP_ARG) {
    const int r15 = lane & 15, g = lane >> 4;
    LDSP f32x4* xch = (LDSP f32x4*)lds + (w & 3) * 6 * 64;
    if constexpr (KH == 1) {
#pragma unroll
        for (int m = 0; m < 2; ++m) { xch[(m * 2) * 64 + lane] = acc[m][0]; xch[(m * 2 + 1) * 64 + lane] = acc[m][1]; }
    } else { xch[4 * 64 + lane] = acc[2][0]; xch[5 * 64 + lane] = acc[2][1]; }
    constexpr int NU = KH ? 1 : 2;
    u32x2 xv[NU][2];
#pragma unroll
    for (int u = 0; u < NU; ++u) {
        const int row = 48 * wr + 16 * (KH ? 2 : u) + r15;
#pragma unroll
        for (int n = 0; n < 2; ++n) xv[u][n] = *(const LDSP u32x2*)(lds + L_X + row * 128 + (((4 * wc + 2 * n + (g >> 1)) ^ (row & 7)) << 4) + (g & 1) * 8);
    }
    BAR_LDS();
    STAMP(26);
    f32x4 other[NU][2];
#pragma unroll
    for (int u = 0; u < NU; ++u)
#pragma unroll
        for (int n = 0; n < 2; ++n) other[u][n] = xch[((KH ? 2 : u) * 2 + n) * 64 + lane];
    const rsrc_t ro = mk_rsrc(P.out, rowlim * 1280);
#pragma unroll
    for (int u = 0; u < NU; ++u) {
        const int m = KH ? 2 : u, t = row0 + 48 * wr + 16 * m + r15;
        unsigned pk[2][2];
#pragma unroll
        for (int n = 0; n < 2; ++n) {
            const f32x4 v = acc[m][n] + other[u][n];
            pk[n][0] = pk2(v[0] + bflo(xv[u][n][0]), v[1] + bfhi(xv[u][n][0])); pk[n][1] = pk2(v[2] + bflo(xv[u][n][1]), v[3] + bfhi(xv[u][n][1]));
        }
        auto x0 = __builtin_amdgcn_permlane16_swap(pk[0][0], pk[1][0], false, false);
        auto x1 = __builtin_amdgcn_permlane16_swap(pk[0][1], pk[1][1], false, false);
        __builtin_amdgcn_raw_buffer_store_b128(u32x4{x0[0], x1[0], x0[1], x1[1]}, ro, (unsigned)(t * D_ + cb * 64 + wc * 32 + 16 * (g & 1) + 8 * (g >> 1)) * 2, 0, K3_AUX);
    }
}

__global__ __launch_bounds__(768, 3) void k3_out(Params P) {
    extern __shared__ __attribute__((aligned(16))) char smem[];
    const int tid = threadIdx.x, lane = tid & 63, w = __builtin_amdgcn_readfirstlane(tid >> 6);
    STAMP_DECL; STAMP(0); RSTAMP(30);
    const int b = blockIdx.x, j0 = b >> 3;
    if (j0 >= 30) return;
    const int rbl = j0 / 10, cb = j0 - rbl * 10;
    const int row0 = (b & 7) * 256 + rbl * BM, rowlim = (b & 7) * 256 + 256;
    lds_ptr lds = (lds_ptr)smem;
    const unsigned lds0 = (unsigned)(uintptr_t)smem;
    if (w >= 8) {
        const int L = w - 8, r8 = lane >> 3, c8 = lane & 7, k4 = lane >> 4;
        Ld ld;
        ld.ra = mk_rsrc(P.ws + OFF_OU, rowlim * 3072); ld.rb0 = mk_rsrc(P.wo, D_ * 1280); ld.rb1 = mk_rsrc(P.wout, FH_ * 1280);
#pragma unroll
        for (int i = 0; i < 2; ++i) {
            ld.va[i] = r8 * 3072 + ((c8 ^ ((4 * ((L + i) & 1) + k4) & 7)) << 4);
            ld.vb[i] = r8 * 1280 + ((c8 ^ (((k4 & 1) | (i << 1)) << 1)) << 4);
        }
        ld.sa = (row0 + 24 * L) * 3072; ld.sb = cb * 128 + 16 * L * 1280;
        ld.da = lds0 + 3 * L * 1024; ld.db = lds0 + A_BYTES + 2 * L * 1024;
        ld.rx = mk_rsrc(P.x, rowlim * 1280); ld.vx = r8 * 1280 + ((c8 ^ r8) << 4); ld.sx = (row0 + 24 * L) * 1280 + cb * 128;
        ld.rot = 0;
        STAMP(1);
        ld_issue<0>(ld); ld_issue<1>(ld); ld_issue<2>(ld);
        STAMP(2);
        WAIT_BAR(10);
        STAMP(3);
        ld_step<0>(ld);
        return;
    }
    const int kh = w >> 2, wr = w & 1, wc = (w >> 1) & 1;
    const int r15 = lane & 15, g = lane >> 4, q = r15 >> 2, p = lane & 3;
    Ctx c;
    const unsigned xoff = (48 * wr + r15) * 128 + (((4 * kh + g) ^ ((r15 >> 1) & 7)) << 4);
    const int fk = (((q >> 1) & 1) | ((g & 1) << 1)) << 1;
    const unsigned woff = A_BYTES + (32 * kh + 8 * g + q) * 128 + (((4 * wc + (p >> 1)) ^ fk) << 4) + 8 * (p & 1);
#pragma unroll
    for (int i = 0; i < 3; ++i) { c.xo[i] = xoff + i * 3 * STAGE; c.wo0[i] = woff + i * 3 * STAGE; c.wo1[i] = (woff ^ 32) + i * 3 * STAGE; }
    f32x4 acc[3][2];
#pragma unroll
    for (int m = 0; m < 3; ++m) { acc[m][0] = f32x4{0, 0, 0, 0}; acc[m][1] = f32x4{0, 0, 0, 0}; }
    Frag f0, f1;
    BAR_LDS();
    rd<0>(f0, lds, c);

    stage<0>(lds, c, f0, f1, acc STAMP_PASS);
    STAMP(28);
    BAR_LDS();
    STAMP(25);
    if (kh) finish<1>(P, lds, acc, w, lane, wr, wc, row0, rowlim, cb STAMP_PASS); else finish<0>(P, lds, acc, w, lane, wr, wc, row0, rowlim, cb STAMP_PASS);
    STAMP(29); RSTAMP(31); STAMP_FLUSH_K(2);
}
}
extern "C" void kernel_launch(void* const* d_in, const int* in_sizes, int n_in, void* d_out, int out_size, void* d_ws, size_t ws_size,
                              hipStream_t stream) {
    (void)in_sizes; (void)n_in; (void)out_size; (void)ws_size;
    const u16* x = (const u16*)d_in[0];
    k1::Params p1{x, (const u16*)d_in[1], (const u16*)d_in[2], (const u16*)d_in[3], (const u16*)d_in[6], (const u16*)d_in[7], (char*)d_ws};
    k2::Params p2{(const u16*)d_in[7], (const u16*)d_in[8], (const u16*)d_in[4], (const u16*)d_in[9], x, (char*)d_ws};
    k3::Params p3{x, (const u16*)d_in[4], (const u16*)d_in[9], (const char*)d_ws, (u16*)d_out};
    (void)hipFuncSetAttribute((const void*)k1::k1_proj, hipFuncAttributeMaxDynamicSharedMemorySize, k1::LDS_BYTES);
    (void)hipFuncSetAttribute((const void*)k2::k2_mix, hipFuncAttributeMaxDynamicSharedMemorySize, k2::LDS_BYTES);
    (void)hipFuncSetAttribute((const void*)k3::k3_out, hipFuncAttributeMaxDynamicSharedMemorySize, k3::LDS_BYTES);
    k1::k1_proj<<<256, 768, k1::LDS_BYTES, stream>>>(p1);
    k2::k2_mix<<<256, 768, k2::LDS_BYTES, stream>>>(p2);
    k3::k3_out<<<256, 768, k3::LDS_BYTES, stream>>>(p3);
}
```
